```python
import torch

def _softmax(x, axis=-1):
    return torch.softmax(x, dim=axis)
def _gelu(x):
    return torch.nn.functional.gelu(x, approximate="tanh")
def _silu(x):
    return torch.nn.functional.silu(x)
def _sigmoid(x):
    return torch.sigmoid(x)
def _layernorm(x, axis=-1, eps=1e-5):
    m = x.mean(dim=axis, keepdim=True)
    v = x.var(dim=axis, keepdim=True, unbiased=False)
    return (x - m) / torch.sqrt(v + eps)
def _scatter_add(tgt, idx, src, axis=0):
    return tgt.clone().index_add_(axis, idx, src)
def _segment_sum(src, seg, n):
    out = torch.zeros((n,) + src.shape[1:], dtype=src.dtype)
    return out.index_add_(0, seg, src)
def _pad(x, pw):
    flat = tuple(v for pair in reversed(pw) for v in pair)
    return torch.nn.functional.pad(x, flat)

kernel_name = 'gated_mix__mha_block_cached__mixer_channel_mlp__decode'

def setup_inputs():

    _w = (lambda a: (lambda t: t.to(torch.bfloat16) if t.is_floating_point() else t)(torch.as_tensor(a)))
    def _A_setup():
        B, Sq, D = 2, 1, 576
        H, Sk, Dh = 32, 4096, 128
        return {
            "x": _w((lambda *s: torch.randn(*s, dtype=torch.bfloat16) * 0.05)(B, Sq, D)),
            "k_cache": _w((lambda *s: torch.randn(*s, dtype=torch.bfloat16) * 6.4)(B, H, Sk, Dh)),
            "v_cache": _w((lambda *s: torch.randn(*s, dtype=torch.bfloat16) * 1.6)(B, H, Sk, Dh)),
            "wq": _w((lambda *s: torch.randn(*s, dtype=torch.bfloat16) * 6.4)(D, H * Dh) * (D ** -0.5)),
            "wo": _w((lambda *s: torch.randn(*s, dtype=torch.bfloat16) * 1.6)(H * Dh, D) * ((H * Dh) ** -0.5)),
        }
    _A_in = _A_setup()

    def _B_setup():
        B, S, D, F = 2, 1, 576, 4608
        return {
            "x": _w((lambda *s: torch.randn(*s, dtype=torch.bfloat16) * 0.05)(B, S, D)),
            "w1": _w((lambda *s: torch.randn(*s, dtype=torch.bfloat16) * 3.2)(D, F) * (D ** -0.5)),
            "b1": _w((lambda *s: torch.randn(*s, dtype=torch.bfloat16) * 1.6)(F)),
            "w2": _w((lambda *s: torch.randn(*s, dtype=torch.bfloat16) * 0.1)(F, D) * (F ** -0.5)),
            "b2": _w((lambda *s: torch.randn(*s, dtype=torch.bfloat16) * 0.1)(D)),
        }
    _B_in = _B_setup()

    return {
        "A_x": _A_in["x"],
        "A_k_cache": _A_in["k_cache"],
        "A_v_cache": _A_in["v_cache"],
        "A_wq": _A_in["wq"],
        "A_wo": _A_in["wo"],
        "B_x": _B_in["x"],
        "B_w1": _B_in["w1"],
        "B_b1": _B_in["b1"],
        "B_w2": _B_in["w2"],
        "B_b2": _B_in["b2"],
        "gate_w": _w((lambda *s: torch.randn(*s, dtype=torch.bfloat16) * 0.05)(576) * 16.0),
    }

def reference(A_x, A_k_cache, A_v_cache, A_wq, A_wo, B_x, B_w1, B_b1, B_w2, B_b2, gate_w):
    def _A_ref(x, k_cache, v_cache, wq, wo):
        B, Sq, D = x.shape
        H, Dh = 32, 128
        q = (lambda x, *d: x.permute(*d))((x @ wq).reshape(B, Sq, H, Dh), 0, 2, 1, 3)
        scores = torch.einsum("bhqd,bhkd->bhqk", q, k_cache) * (Dh ** -0.5)
        p = _softmax(scores, axis=-1)
        o = torch.einsum("bhqk,bhkd->bhqd", p, v_cache)
        return (lambda x, *d: x.permute(*d))(o, 0, 2, 1, 3).reshape(B, Sq, H * Dh) @ wo

    def _B_ref(x, w1, b1, w2, b2):
        y = _layernorm(x, axis=-1)
        h = _gelu(torch.einsum("bsd,df->bsf", y, w1) + b1)
        return torch.einsum("bsf,fd->bsd", h, w2) + b2

    x = A_x
    g = _sigmoid((x * gate_w).sum(-1, keepdims=True))
    a = _A_ref(x, A_k_cache, A_v_cache, A_wq, A_wo)
    b = _B_ref(x, B_w1, B_b1, B_w2, B_b2)
    return g * a + (1 - g) * b
```

```cpp
#include <hip/hip_runtime.h>
#include <hip/hip_bf16.h>
#include <stdint.h>
typedef unsigned short u16;
using u32x4 = __attribute__((ext_vector_type(4))) unsigned;
__device__ __forceinline__ float bf2f(u16 v){ return __uint_as_float(((unsigned)v)<<16); }
__device__ __forceinline__ float lo(unsigned u){ return __uint_as_float(u<<16);}
__device__ __forceinline__ float hi(unsigned u){ return __uint_as_float(u&0xffff0000u);}
constexpr int D=576,H=32,SK=4096,DH=128,F=4608;
__global__ void __launch_bounds__(256) k1(const u16* x,const u16* wq,const u16* w1,const u16* b1,float* q,float* hb){
  __shared__ float xs[2][D]; __shared__ float red[4][2][64]; __shared__ float st[4];
  int t=threadIdx.x; bool isq = blockIdx.x<64;
  for(int i=t;i<2*D;i+=256) xs[i/D][i%D]=bf2f(x[i]);
  __syncthreads();
  if(!isq){
    if(t<2){ float m=0; for(int i=0;i<D;i++) m+=xs[t][i]; m/=D; float v=0; for(int i=0;i<D;i++){float d=xs[t][i]-m; v+=d*d;} v/=D; st[t*2]=m; st[t*2+1]=1.f/sqrtf(v+1e-5f);}
    __syncthreads();
    for(int i=t;i<2*D;i+=256){int b=i/D; xs[b][i%D]=(xs[b][i%D]-st[b*2])*st[b*2+1];}
    __syncthreads();
  }
  const u16* W = isq? wq : w1; int N = isq? 4096: F; int c0 = (isq? blockIdx.x : blockIdx.x-64)*64;
  int w=t>>6,l=t&63; float a0=0,a1=0;
  for(int r=w;r<D;r+=4){ float wv=bf2f(W[(size_t)r*N+c0+l]); a0+=xs[0][r]*wv; a1+=xs[1][r]*wv; }
  red[w][0][l]=a0; red[w][1][l]=a1; __syncthreads();
  if(t<128){ int b=t>>6; float s=red[0][b][l]+red[1][b][l]+red[2][b][l]+red[3][b][l];
    if(isq) q[b*4096+c0+l]=s; else { float z=s+bf2f(b1[c0+l]); float g=0.5f*z*(1.f+tanhf(0.7978845608f*(z+0.044715f*z*z*z))); hb[b*F+c0+l]=g; } }
}
constexpr int NS=8, KPS=SK/NS;
__global__ void __launch_bounds__(256) k2(const u16* K,const u16* V,const float* q,float* po,float* pml){
  __shared__ float sc[KPS]; __shared__ float red[16][DH]; __shared__ float r4[8];
  int bh=blockIdx.x/NS, s=blockIdx.x%NS, t=threadIdx.x; int b=bh/H,h=bh%H;
  int c=t&15, r=t>>4; float qv[8];
  for(int i=0;i<8;i++) qv[i]=q[b*4096+h*DH+c*8+i]*0.08838834764831845f;
  const u16* Kp=K+((size_t)bh*SK+s*KPS)*DH; const u16* Vp=V+((size_t)bh*SK+s*KPS)*DH;
  #pragma unroll 8
  for(int k=r;k<KPS;k+=16){ u32x4 u=__builtin_nontemporal_load((const u32x4*)(Kp+(size_t)k*DH+c*8)); float d=0;
    for(int i=0;i<4;i++){ d+=lo(u[i])*qv[2*i]+hi(u[i])*qv[2*i+1]; }
    d+=__shfl_xor(d,1); d+=__shfl_xor(d,2); d+=__shfl_xor(d,4); d+=__shfl_xor(d,8); if(c==0) sc[k]=d; }
  __syncthreads();
  float m=-INFINITY; for(int k=t;k<KPS;k+=256) m=fmaxf(m,sc[k]);
  for(int o=1;o<64;o<<=1) m=fmaxf(m,__shfl_xor(m,o)); if((t&63)==0) r4[t>>6]=m; __syncthreads();
  m=fmaxf(fmaxf(r4[0],r4[1]),fmaxf(r4[2],r4[3]));
  float l=0; for(int k=t;k<KPS;k+=256){ float p=__expf(sc[k]-m); sc[k]=p; l+=p; }
  for(int o=1;o<64;o<<=1) l+=__shfl_xor(l,o); if((t&63)==0) r4[4+(t>>6)]=l; __syncthreads();
  l=r4[4]+r4[5]+r4[6]+r4[7];
  float acc[8]={0,0,0,0,0,0,0,0};
  #pragma unroll 8
  for(int k=r;k<KPS;k+=16){ u32x4 u=__builtin_nontemporal_load((const u32x4*)(Vp+(size_t)k*DH+c*8)); float p=sc[k];
    for(int i=0;i<4;i++){ acc[2*i]+=p*lo(u[i]); acc[2*i+1]+=p*hi(u[i]); } }
  for(int i=0;i<8;i++) red[r][c*8+i]=acc[i]; __syncthreads();
  if(t<DH){ float o=0; for(int i=0;i<16;i++) o+=red[i][t]; po[(size_t)blockIdx.x*DH+t]=o; }
  if(t==0){ pml[blockIdx.x*2]=m; pml[blockIdx.x*2+1]=l; }
}
__global__ void __launch_bounds__(576) k3(const u16* x,const u16* gw,const float* po,const float* pml,const float* hb,const u16* wo,const u16* w2,float* part){
  __shared__ float v[2][34]; __shared__ float g[2]; int t=threadIdx.x;
  if(t<128){ int b=t>>6,l=t&63; float s=0; for(int i=l;i<D;i+=64) s+=bf2f(x[b*D+i])*bf2f(gw[i]); for(int o=1;o<64;o<<=1) s+=__shfl_xor(s,o); if(l==0) g[b]=1.f/(1.f+__expf(-s)); }
  __syncthreads();
  if(t<68){ int b=t/34,i=t%34; int row=blockIdx.x*34+i; float val;
    if(row<4096){ int h=row/DH,d=row%DH; int bh=b*H+h; float M=-INFINITY; for(int s=0;s<NS;s++) M=fmaxf(M,pml[(bh*NS+s)*2]); float L=0,o=0; for(int s=0;s<NS;s++){ float w=__expf(pml[(bh*NS+s)*2]-M); L+=w*pml[(bh*NS+s)*2+1]; o+=w*po[(size_t)(bh*NS+s)*DH+d]; } val=g[b]*o/L; }
    else val=(1.f-g[b])*hb[b*F+row-4096];
    v[b][i]=val; }
  __syncthreads();
  float a0=0,a1=0;
  for(int i=0;i<34;i++){ int row=blockIdx.x*34+i; float w= row<4096? bf2f(wo[(size_t)row*D+t]) : bf2f(w2[(size_t)(row-4096)*D+t]); a0+=v[0][i]*w; a1+=v[1][i]*w; }
  part[(size_t)blockIdx.x*1152+t]=a0; part[(size_t)blockIdx.x*1152+576+t]=a1;
}
__global__ void __launch_bounds__(256) k4(const u16* x,const u16* gw,const float* part,const u16* b2,__hip_bfloat16* out){
  __shared__ float red[16][16]; __shared__ float g[2]; int t=threadIdx.x;
  if(t<128){ int b=t>>6,l=t&63; float s=0; for(int i=l;i<D;i+=64) s+=bf2f(x[b*D+i])*bf2f(gw[i]); for(int o=1;o<64;o<<=1) s+=__shfl_xor(s,o); if(l==0) g[b]=1.f/(1.f+__expf(-s)); }
  int c=blockIdx.x*16+(t&15), r=t>>4; float s=0; for(int i=r;i<256;i+=16) s+=part[(size_t)i*1152+c];
  red[r][t&15]=s; __syncthreads();
  if(t<16){ float o=0; for(int i=0;i<16;i++) o+=red[i][t]; int b=c/576; o+=(1.f-g[b])*bf2f(b2[c%576]); out[c]=__float2bfloat16(o);}
}
extern "C" void kernel_launch(void* const* d_in,const int* in_sizes,int n_in,void* d_out,int out_size,void* d_ws,size_t ws_size,hipStream_t stream){
  const u16* x=(const u16*)d_in[0]; const u16* K=(const u16*)d_in[1]; const u16* V=(const u16*)d_in[2]; const u16* wq=(const u16*)d_in[3]; const u16* wo=(const u16*)d_in[4];
  const u16* w1=(const u16*)d_in[6]; const u16* b1=(const u16*)d_in[7]; const u16* w2=(const u16*)d_in[8]; const u16* b2=(const u16*)d_in[9]; const u16* gw=(const u16*)d_in[10];
  float* ws=(float*)d_ws; float* q=ws; float* hb=q+8192; float* po=hb+2*F; float* pml=po+512*DH; float* part=pml+1024;
  k1<<<64+72,256,0,stream>>>(x,wq,w1,b1,q,hb);
  k2<<<64*NS,256,0,stream>>>(K,V,q,po,pml);
  k3<<<256,576,0,stream>>>(x,gw,po,pml,hb,wo,w2,part);
  k4<<<72,256,0,stream>>>(x,gw,part,b2,(__hip_bfloat16*)d_out);
}
```

```cpp
#include <hip/hip_runtime.h>
#include <hip/hip_bf16.h>
#include <stdint.h>

typedef unsigned u32; typedef unsigned long long u64; typedef unsigned short u16;
typedef u32   u32x4 __attribute__((ext_vector_type(4)));
typedef u32   u32x2 __attribute__((ext_vector_type(2)));
typedef float f32x4 __attribute__((ext_vector_type(4)));
typedef short s16x4 __attribute__((ext_vector_type(4)));
typedef __bf16 bf16x2 __attribute__((ext_vector_type(2)));
typedef __bf16 bf16x8 __attribute__((ext_vector_type(8)));
#define LAS __attribute__((address_space(3)))
#define GAS __attribute__((address_space(1)))

#define WIN 8
#define RING 32
#define TRIG 127
#define KV_AUX 2
#define W_NT ""

constexpr int DM = 576, FF = 4608;
constexpr size_t WS_EP = 0, WS_QP = 4096, WS_HP = WS_QP + 32 * 8 * 256 * 8, WS_ST = WS_HP + 32 * 8 * 288 * 8,
                 WS_OP = WS_ST + 64 * 64 * 8, WS_FP = WS_OP + 256 * 128 * 8, WS_END = WS_FP + 576 * 64 * 8;
constexpr int L_WQ = 0, L_W1 = 18432, L_WF = L_W1 + 21504, L_Q = L_WF + 40960, L_ACC = L_Q + 256, L_A2 = L_ACC + 2048,
              L_AO = L_A2 + 4 * 320, L_END = L_AO + 4 * 256;
constexpr u32 SPIN_CAP = 1u << 20;

__device__ __forceinline__ float bflo(u32 u) { return __uint_as_float(u << 16); }
__device__ __forceinline__ float bfhi(u32 u) { return __uint_as_float(u & 0xffff0000u); }
__device__ __forceinline__ u32 cvtpk(float a, float b) { bf16x2 c = {(__bf16)a, (__bf16)b}; return __builtin_bit_cast(u32, c); }
template <int C> __device__ __forceinline__ u32 dppu(u32 x) { return (u32)__builtin_amdgcn_update_dpp(0, (int)x, C, 0xf, 0xf, true); }
template <int C> __device__ __forceinline__ float dppf(float x) { return __uint_as_float(dppu<C>(__float_as_uint(x))); }
constexpr int X1 = 0xB1, X2 = 0x4E, X7 = 0x141, R8 = 0x128, R4 = 0x124, R2 = 0x122, R1 = 0x121;
__device__ __forceinline__ float sw16s(float x) { auto s = __builtin_amdgcn_permlane16_swap(__float_as_uint(x), __float_as_uint(x), false, false); return __uint_as_float(s[0]) + __uint_as_float(s[1]); }
__device__ __forceinline__ float sw32s(float x) { auto s = __builtin_amdgcn_permlane32_swap(__float_as_uint(x), __float_as_uint(x), false, false); return __uint_as_float(s[0]) + __uint_as_float(s[1]); }
__device__ __forceinline__ float sw16m(float x) { auto s = __builtin_amdgcn_permlane16_swap(__float_as_uint(x), __float_as_uint(x), false, false); return fmaxf(__uint_as_float(s[0]), __uint_as_float(s[1])); }
__device__ __forceinline__ float sw32m(float x) { auto s = __builtin_amdgcn_permlane32_swap(__float_as_uint(x), __float_as_uint(x), false, false); return fmaxf(__uint_as_float(s[0]), __uint_as_float(s[1])); }
__device__ __forceinline__ float row16s(float x) { x += dppf<X1>(x); x += dppf<X2>(x); x += dppf<X7>(x); x += dppf<R8>(x); return x; }
__device__ __forceinline__ float row16m(float x) { x = fmaxf(x, dppf<X1>(x)); x = fmaxf(x, dppf<X2>(x)); x = fmaxf(x, dppf<X7>(x)); x = fmaxf(x, dppf<R8>(x)); return x; }
__device__ __forceinline__ float rdl(float x, int l) { return __uint_as_float(__builtin_amdgcn_readlane(__float_as_uint(x), l)); }
__device__ __forceinline__ float dot2(u32 a, u32 b, float c) { return __builtin_amdgcn_fdot2_f32_bf16(__builtin_bit_cast(bf16x2, a), __builtin_bit_cast(bf16x2, b), c, false); }
__device__ __forceinline__ f32x4 mfma(u32x4 a, u32x4 b, f32x4 c) { return __builtin_amdgcn_mfma_f32_16x16x32_bf16(__builtin_bit_cast(bf16x8, a), __builtin_bit_cast(bf16x8, b), c, 0, 0, 0); }
__device__ __forceinline__ void wg_barrier() { asm volatile("s_waitcnt lgkmcnt(0)\n\ts_barrier" ::: "memory"); }
__device__ __forceinline__ void vm_drain() { asm volatile("s_waitcnt vmcnt(0)" ::: "memory"); }
__device__ __forceinline__ void dma16(const void* base, u32 voff, u32 lds) {
  u32 keep;
  asm volatile("s_mov_b32 %0, m0\n\ts_mov_b32 m0, %3\n\ts_nop 0\n\tglobal_load_lds_dwordx4 %1, %2" W_NT "\n\ts_mov_b32 m0, %0"
               : "=&s"(keep) : "v"(voff), "s"(base), "s"(lds) : "memory");
}
__device__ __forceinline__ u32x4 trb(LAS char* p, int rowbytes) {
  s16x4 a = __builtin_amdgcn_ds_read_tr16_b64_v4i16((LAS s16x4*)p);
  s16x4 b = __builtin_amdgcn_ds_read_tr16_b64_v4i16((LAS s16x4*)(p + 4 * rowbytes));
  u32x2 x = __builtin_bit_cast(u32x2, a), y = __builtin_bit_cast(u32x2, b);
  return u32x4{x[0], x[1], y[0], y[1]};
}
struct Tag { u32 lo, hi; };
__device__ __forceinline__ void put_raw(GAS u64* p, u32 bits24, Tag t) {
  __hip_atomic_store(p, ((u64)t.hi << 32) | (u64)(bits24 | t.lo), __ATOMIC_RELAXED, __HIP_MEMORY_SCOPE_AGENT);
}
__device__ __forceinline__ void put(GAS u64* p, float v, Tag t) { put_raw(p, (__float_as_uint(v) + 0x80u) & 0xffffff00u, t); }
template <int N, int SLEEP> __device__ __forceinline__ void gather(GAS u64* p, int stride, Tag t, float (&v)[N]) {
  for (u32 spin = 0;; ++spin) {
    u32 bad = 0;
#pragma unroll
    for (int k = 0; k < N; ++k) {
      const u64 x = __hip_atomic_load(p + k * stride, __ATOMIC_RELAXED, __HIP_MEMORY_SCOPE_AGENT);
      bad |= ((u32)(x >> 32) ^ t.hi) | (((u32)x ^ t.lo) & 0xffu);
      v[k] = __uint_as_float((u32)x & 0xffffff00u);
    }
    if (__builtin_amdgcn_ballot_w64(bad != 0) == 0) break;
    if (spin > SPIN_CAP) { v[0] = __uint_as_float(0x7fc00000u); break; }
    if (SLEEP) __builtin_amdgcn_s_sleep(SLEEP);
  }
}
#define STAMP(i)
#define STAMP_OUT()

__global__ void __launch_bounds__(512, 2)
fused_kernel(const u16* __restrict__ x, const u16* __restrict__ kc, const u16* __restrict__ vc, const u16* __restrict__ wq,
             const u16* __restrict__ wo, const u16* __restrict__ w1, const u16* __restrict__ b1, const u16* __restrict__ w2,
             const u16* __restrict__ b2, const u16* __restrict__ gw, u16* __restrict__ out, char* ws) {
  __shared__ __attribute__((aligned(16))) char lds_raw[L_END];
  LAS char* lds = (LAS char*)lds_raw;
  const u32 lds0 = (u32)(size_t)lds;
  const int tid = threadIdx.x, lane = tid & 63, wave = __builtin_amdgcn_readfirstlane(tid >> 6);
  const int bid = blockIdx.x, idx = bid >> 3, h = (bid & 7) * 4 + (idx >> 3), j = idx & 7, b = j >> 2, s = j & 3;
  const int g4 = lane >> 4, c16 = lane & 15;
  GAS char* wsg = (GAS char*)ws;
  STAMP(0);
  const u64 ep = __hip_atomic_load((GAS u64*)(wsg + WS_EP), __ATOMIC_RELAXED, __HIP_MEMORY_SCOPE_AGENT);
  const int trl = (8 * g4 + (c16 >> 2)), trc = (lane & 3) * 8;

  if (wave >= 4) {
    const int sw = wave - 4;
    const size_t slab = ((size_t)(b * 32 + h) * 4 + s) * 262144;
    const __amdgpu_buffer_rsrc_t rk = __builtin_amdgcn_make_buffer_rsrc((void*)((const char*)kc + slab), 0, 262144, 0x00020000);
    const __amdgpu_buffer_rsrc_t rv = __builtin_amdgcn_make_buffer_rsrc((void*)((const char*)vc + slab), 0, 262144, 0x00020000);
    const int vo = sw * 1024 + lane * 16;
    int so = idx * 4096;
    u32x4 ring[RING];
#define ISSUE(n) do { if ((n) >= WIN) asm volatile("s_waitcnt vmcnt(%0)" :: "i"(WIN - 1) : "memory"); \
      ring[(n) % RING] = __builtin_amdgcn_raw_buffer_load_b128((n) < 64 ? rk : rv, vo, so, KV_AUX); \
      so = (so + 4096) & 0x3ffff; asm volatile("" : "+s"(so)); __builtin_amdgcn_sched_barrier(0); } while (0)
#pragma unroll
    for (int n = 0; n < WIN; ++n) ISSUE(n);
    wg_barrier();
    STAMP(1);
#pragma unroll
    for (int n = WIN; n < RING; ++n) ISSUE(n);
    const u64 epn = ep + 1;
    const Tag tag = {(u32)__builtin_amdgcn_readfirstlane((u32)(epn >> 32) & 0xffu), (u32)__builtin_amdgcn_readfirstlane((u32)epn)};
    STAMP(2);
    wg_barrier();
    STAMP(3);
    const u32x4 q = *(LAS u32x4*)(lds + L_Q + c16 * 16);
    float sc[4], t8[8];
#pragma unroll
    for (int n = 0; n < 64; ++n) {
      const u32x4 k = ring[n % RING];
      float d = dot2(k[0], q[0], 0.f); d = dot2(k[1], q[1], d); d = dot2(k[2], q[2], d); d = dot2(k[3], q[3], d);
      if (n + RING < 128) ISSUE(n + RING);
      if (n + RING == TRIG) wg_barrier();
      const int i = n & 15;
      d += dppf<R8>(d);
      if (i < 8) t8[i] = d; else t8[i - 8] = (c16 & 8) ? d : t8[i - 8];
      if (i == 15) {
#pragma unroll
        for (int u = 0; u < 4; ++u) { const float lo = t8[u] + dppf<X7>(t8[u]), hi = t8[u + 4] + dppf<X7>(t8[u + 4]); t8[u] = (c16 & 4) ? hi : lo; }
#pragma unroll
        for (int u = 0; u < 2; ++u) { const float lo = t8[u] + dppf<X2>(t8[u]), hi = t8[u + 2] + dppf<X2>(t8[u + 2]); t8[u] = (c16 & 2) ? hi : lo; }
        { const float lo = t8[0] + dppf<X1>(t8[0]), hi = t8[1] + dppf<X1>(t8[1]); sc[n >> 4] = ((c16 & 1) ? hi : lo) * (0.08838834764831845f * 1.4426950408889634f); }
      }
      __builtin_amdgcn_sched_barrier(0);
    }
    STAMP(4);
    float m = fmaxf(fmaxf(sc[0], sc[1]), fmaxf(sc[2], sc[3]));
    m = sw32m(sw16m(row16m(m)));
    u32 pk[4]; float l = 0.f;
#pragma unroll
    for (int u = 0; u < 4; ++u) { const float p = __builtin_amdgcn_exp2f(sc[u] - m); pk[u] = cvtpk(p, p); l += bflo(pk[u]); }
    l = sw32s(sw16s(row16s(l)));
    if (lane < 4) {
      const u32 mb = __float_as_uint(m);
      const u32 v = lane == 0 ? (mb & 0xffffff00u) : lane == 1 ? (mb & 0xffu) << 8 : (__float_as_uint(l) + 0x80u) & 0xffffff00u;
      put_raw((GAS u64*)(wsg + WS_ST) + (b * 32 + h) * 64 + (s * 4 + sw) * 4 + lane, v, tag);
    }
    u32 mk[4];
#pragma unroll
    for (int u = 0; u < 4; ++u) mk[u] = (c16 >> 1) == u ? ((c16 & 1) ? 0xffff0000u : 0x0000ffffu) : 0u;
    f32x4 acc = {0.f, 0.f, 0.f, 0.f};
#define PV(n) do { const u32 pb_ = pk[((n) - 64) >> 4]; constexpr int C_ = 0x150 + (((n) - 64) & 15); \
      const u32x4 B_ = {dppu<C_>(pb_) & mk[0], dppu<C_>(pb_) & mk[1], dppu<C_>(pb_) & mk[2], dppu<C_>(pb_) & mk[3]}; \
      acc = mfma(ring[(n) % RING], B_, acc); __builtin_amdgcn_sched_barrier(0); \
      if ((n) + RING < 128) ISSUE((n) + RING); if ((n) + RING == TRIG) { STAMP(5); wg_barrier(); } } while (0)
#define PV4(n) PV(n); PV((n) + 1); PV((n) + 2); PV((n) + 3)
#define PV16(n) PV4(n); PV4((n) + 4); PV4((n) + 8); PV4((n) + 12)
    PV16(64); PV16(80); PV16(96); PV16(112);
    if (c16 < 8) *(LAS f32x4*)(lds + L_ACC + sw * 512 + (g4 * 8 + c16) * 16) = acc;
    STAMP(6);
    wg_barrier();
    wg_barrier();
    STAMP(7);
    f32x4 c0 = {0.f, 0.f, 0.f, 0.f}, c1 = c0;
    LAS char* wb = lds + L_WF + trl * 144 + trc + sw * 32;
    LAS char* wb4 = lds + L_WF + trl * 144 + trc + 128;
#pragma unroll
    for (int ks = 0; ks < 5; ++ks) {
      const u32x4 a = *(LAS u32x4*)(lds + L_A2 + (lane & 3) * 320 + ks * 64 + g4 * 16);
      c0 = mfma(a, trb(wb + ks * 32 * 144, 144), c0);
      if (sw == 0) c1 = mfma(a, trb(wb4 + ks * 32 * 144, 144), c1);
    }
    u32x4 bo[4], bo4[4];
#pragma unroll
    for (int ks = 0; ks < 4; ++ks) { bo[ks] = trb(wb + (144 + ks * 32) * 144, 144); bo4[ks] = trb(wb4 + (144 + ks * 32) * 144, 144); }
    wg_barrier();
    STAMP(8);
#pragma unroll
    for (int ks = 0; ks < 4; ++ks) {
      const u32x4 a = *(LAS u32x4*)(lds + L_AO + (lane & 3) * 256 + ks * 64 + g4 * 16);
      c0 = mfma(a, bo[ks], c0);
      c1 = mfma(a, bo4[ks], c1);
    }
    {
      const f32x4 c = lane < 32 ? c0 : c1;
      const float v = (g4 & 1) ? c[1] + c[3] : c[0] + c[2];
      const int col = j * 72 + (lane < 32 ? sw * 16 : 64) + c16;
      if (lane < 32 || (sw == 0 && c16 < 8)) put((GAS u64*)(wsg + WS_FP) + col * 64 + h * 2 + (g4 & 1), v, tag);
    }
    STAMP(9);
    STAMP_OUT();
    return;
  }

  const int hw = wave, t = tid;
#pragma unroll
  for (int i = 0; i < 5; ++i) {
    const int u = hw + 4 * i;
    if (u < 18) dma16(wq, ((72 * j + 4 * u + g4) * 4096 + 128 * h + 8 * c16) * 2, lds0 + L_WQ + u * 1024);
  }
  u32 xs[9], gs[9];
#pragma unroll
  for (int i = 0; i < 9; ++i) { xs[i] = ((const u32*)x)[(lane >> 5) * 288 + (lane & 31) + 32 * i]; gs[i] = ((const u32*)gw)[(lane & 31) + 32 * i]; }
  u32x4 xa[3];
#pragma unroll
  for (int ks = 0; ks < 3; ++ks) xa[ks] = *(const u32x4*)(x + (lane & 1) * DM + 72 * j + (ks < 2 ? 32 * ks + 8 * g4 : 64));
  const int f0 = t < 144 ? t : t - 144, f1 = 112 + (t & 31);
  const float bias0 = bflo(b1[144 * h + f0]), bias1 = bflo(b1[144 * h + f1]);
  const int ncol = bid + 256 * hw;
  const float bias2 = bflo(b2[ncol < DM ? ncol : 0]);
  if (t < 32) *(LAS u32*)(lds + L_A2 + (t >> 3) * 320 + 288 + (t & 7) * 4) = 0u;
  float sum = 0.f, gd = 0.f;
#pragma unroll
  for (int i = 0; i < 9; ++i) { sum += bflo(xs[i]) + bfhi(xs[i]); gd = dot2(xs[i], gs[i], gd); }
  sum = sw16s(row16s(sum)); gd = sw16s(row16s(gd));
  const float mean = sum * (1.f / DM);
  float var = 0.f;
#pragma unroll
  for (int i = 0; i < 9; ++i) { const float a = bflo(xs[i]) - mean, c = bfhi(xs[i]) - mean; var += a * a + c * c; }
  var = sw16s(row16s(var));
  const float rstd = 1.f / sqrtf(var * (1.f / DM) + 1e-5f), gate = 1.f / (1.f + __expf(-gd));
  const float mean0 = rdl(mean, 0), mean1 = rdl(mean, 32), rstd0 = rdl(rstd, 0), rstd1 = rdl(rstd, 32), gate0 = rdl(gate, 0), gate1 = rdl(gate, 32);
  const u64 epn = ep + 1;
  const Tag tag = {(u32)__builtin_amdgcn_readfirstlane((u32)(epn >> 32) & 0xffu), (u32)__builtin_amdgcn_readfirstlane((u32)epn)};
  u32x4 aq[3], ay[3];
  {
    const float mu = (lane & 1) ? mean1 : mean0, rs = (lane & 1) ? rstd1 : rstd0;
#pragma unroll
    for (int ks = 0; ks < 3; ++ks) {
      const bool live = ks < 2 || g4 == 0;
#pragma unroll
      for (int e = 0; e < 4; ++e) {
        const float y0 = (bflo(xa[ks][e]) - mu) * rs, y1 = (bfhi(xa[ks][e]) - mu) * rs;
        const u32 hi = cvtpk(y0, y1), lo = cvtpk(y0 - bflo(hi), y1 - bfhi(hi));
        ay[ks][e] = live ? ((lane & 2) ? lo : hi) : 0u;
        aq[ks][e] = (live && !(lane & 2)) ? xa[ks][e] : 0u;
      }
    }
  }
  vm_drain();
  wg_barrier();
  STAMP(1);
  const int trl2 = 64 + (c16 >> 2);
#pragma unroll
  for (int i = 0; i < 2; ++i) {
    const int tl = hw * 2 + i;
    LAS char* p = lds + L_WQ + trc + tl * 32;
    f32x4 c = {0.f, 0.f, 0.f, 0.f};
    c = mfma(aq[0], trb(p + trl * 256, 256), c); c = mfma(aq[1], trb(p + (32 + trl) * 256, 256), c); c = mfma(aq[2], trb(p + trl2 * 256, 256), c);
    if (lane < 32) put((GAS u64*)(wsg + WS_QP) + ((h * 8 + j) * 2 + g4) * 128 + tl * 16 + c16, g4 ? c[1] : c[0], tag);
  }
  STAMP(2);
#pragma unroll
  for (int i = 0; i < 6; ++i) {
    const int u = hw + 4 * i;
    if (u < 21) { const int n = min(u * 64 + lane, 1295), r = n / 18, c = n - r * 18;
      dma16(w1, ((72 * j + r) * FF + 144 * h + 8 * c) * 2, lds0 + L_W1 + u * 1024); }
  }
  {
    float v[4];
    gather<4, 2>((GAS u64*)(wsg + WS_QP) + ((h * 8 + (t & 1) * 4) * 2 + b) * 128 + (t >> 1), 256, tag, v);
    float qv = (v[0] + v[1]) + (v[2] + v[3]);
    qv += dppf<X1>(qv);
    if (!(t & 1)) *(LAS u16*)(lds + L_Q + t) = (u16)cvtpk(qv, qv);
  }
  vm_drain();
  STAMP(3);
  wg_barrier();
#pragma unroll
  for (int i = 0; i < 3; ++i) {
    const int tl = hw + 4 * i;
    if (tl < 9) {
      LAS char* p = lds + L_W1 + trc + tl * 32;
      f32x4 c = {0.f, 0.f, 0.f, 0.f};
      c = mfma(ay[0], trb(p + trl * 288, 288), c); c = mfma(ay[1], trb(p + (32 + trl) * 288, 288), c); c = mfma(ay[2], trb(p + trl2 * 288, 288), c);
      if (lane < 32) put((GAS u64*)(wsg + WS_HP) + (h * 8 + j) * 288 + g4 * 144 + tl * 16 + c16, g4 ? c[1] + c[3] : c[0] + c[2], tag);
    }
  }
  __builtin_amdgcn_s_sleep(60);
#pragma unroll
  for (int i = 0; i < 2; ++i) {
    if (i == 0 || hw == 0) {
      float v[8];
      const int o = i == 0 ? t : 256 + (t & 31);
      gather<8, 16>((GAS u64*)(wsg + WS_HP) + h * 8 * 288 + o, 288, tag, v);
      const float z = (((v[0] + v[1]) + (v[2] + v[3])) + ((v[4] + v[5]) + (v[6] + v[7]))) + (i == 0 ? bias0 : bias1);
      const int bb = o >= 144;
      const float u2 = 1.5957691216057308f * (z + 0.044715f * z * z * z);
      const float a = (1.f - (bb ? gate1 : gate0)) * (z / (1.f + __expf(-u2)));
      const u32 hi = cvtpk(a, a), lo = cvtpk(a - bflo(hi), 0.f);
      LAS char* p = lds + L_A2 + bb * 320 + (i == 0 ? f0 : f1) * 2;
      *(LAS u16*)p = (u16)hi; *(LAS u16*)(p + 640) = (u16)lo;
    }
  }
  STAMP(4);
  float fw[4];
  {
    float v[1];
    gather<1, 16>((GAS u64*)(wsg + WS_ST) + (b * 32 + h) * 64 + lane, 1, tag, v);
    const u32 r = __float_as_uint(v[0]);
    const float mw = __uint_as_float(dppu<0x00>(r) | (dppu<0x55>(r) >> 8)), lw = __uint_as_float(dppu<0xAA>(r));
    float M = fmaxf(mw, dppf<R4>(mw)); M = fmaxf(M, dppf<R8>(M)); M = sw32m(sw16m(M));
    const float e = __builtin_amdgcn_exp2f(mw - M);
    float L = lw * e; L += dppf<X7>(L); L += dppf<R8>(L); L = sw32s(sw16s(L));
    const float f = e * (b ? gate1 : gate0) / L;
#pragma unroll
    for (int u = 0; u < 4; ++u) fw[u] = rdl(f, s * 16 + u * 4);
  }
  STAMP(5);
  wg_barrier();
#pragma unroll
  for (int i = 0; i < 10; ++i) {
    const int u = hw + 4 * i;
    const int n = min(u * 64 + lane, 2447), r = n / 9, c = n - r * 9;
    const u32 o2 = ((144 * h + r) * DM + 72 * j + 8 * c) * 2, oo = ((128 * h + r - 144) * DM + 72 * j + 8 * c) * 2;
    if (u < 20) dma16(w2, o2, lds0 + L_WF + u * 1024);
    else if (u == 20) { if (lane < 16) dma16(w2, o2, lds0 + L_WF + u * 1024); else dma16(wo, oo, lds0 + L_WF + u * 1024); }
    else if (u < 39) dma16(wo, oo, lds0 + L_WF + u * 1024);
  }
  STAMP(6);
  wg_barrier();
  if (hw < 2) {
    LAS float* a = (LAS float*)(lds + L_ACC) + t;
    const float o = (fw[0] * a[0] + fw[1] * a[128]) + (fw[2] * a[256] + fw[3] * a[384]);
    put((GAS u64*)(wsg + WS_OP) + (h * 8 + j) * 128 + t, o, tag);
  }
  STAMP(7);
  vm_drain();
  wg_barrier();
  STAMP(8);
  {
    float v[4];
    const int e = t & 127, bb = t >> 7;
    gather<4, 0>((GAS u64*)(wsg + WS_OP) + (h * 8 + bb * 4) * 128 + e, 128, tag, v);
    const float o = (v[0] + v[1]) + (v[2] + v[3]);
    const u32 hi = cvtpk(o, o), lo = cvtpk(o - bflo(hi), 0.f);
    const int dim = (e & 96) + (e & 3) * 8 + ((e >> 2) & 7);
    LAS char* p = lds + L_AO + bb * 256 + dim * 2;
    *(LAS u16*)p = (u16)hi; *(LAS u16*)(p + 512) = (u16)lo;
  }
  STAMP(9);
  wg_barrier();
  if (ncol < DM) {
    float v[1];
    gather<1, 0>((GAS u64*)(wsg + WS_FP) + ncol * 64 + lane, 1, tag, v);
    float r = v[0];
    r += dppf<X2>(r); r += dppf<R4>(r); r += dppf<R8>(r); r = sw32s(sw16s(r));
    r += (1.f - ((lane & 1) ? gate1 : gate0)) * bias2;
    if (lane < 2) out[lane * DM + ncol] = (u16)cvtpk(r, r);
    if (ncol == 0 && lane == 0) __hip_atomic_store((GAS u64*)(wsg + WS_EP), epn, __ATOMIC_RELAXED, __HIP_MEMORY_SCOPE_AGENT);
  }
  STAMP(10);
  STAMP_OUT();
}

extern "C" void kernel_launch(void* const* d_in, const int* in_sizes, int n_in, void* d_out, int out_size, void* d_ws,
                              size_t ws_size, hipStream_t stream) {
  fused_kernel<<<256, 512, 0, stream>>>((const u16*)d_in[0], (const u16*)d_in[1], (const u16*)d_in[2], (const u16*)d_in[3],
                                        (const u16*)d_in[4], (const u16*)d_in[6], (const u16*)d_in[7], (const u16*)d_in[8],
                                        (const u16*)d_in[9], (const u16*)d_in[10], (u16*)d_out, (char*)d_ws);
}
```

```cpp
#define STAMPS
#define PA_R 1
#define PA_K 4
#define PA_Q 2
#define PB_R 1
#define PB_K 4
#define PB_Q 1
#include <hip/hip_runtime.h>
#include <hip/hip_bf16.h>
#include <stdint.h>

typedef unsigned u32; typedef unsigned long long u64; typedef unsigned short u16;
typedef u32   u32x4 __attribute__((ext_vector_type(4)));
typedef u32   u32x2 __attribute__((ext_vector_type(2)));
typedef float f32x4 __attribute__((ext_vector_type(4)));
typedef short s16x4 __attribute__((ext_vector_type(4)));
typedef __bf16 bf16x2 __attribute__((ext_vector_type(2)));
typedef __bf16 bf16x8 __attribute__((ext_vector_type(8)));
#define LAS __attribute__((address_space(3)))
#define GAS __attribute__((address_space(1)))

#define WIN 8
#define RING 32
#define TRIG 127
#define KV_AUX 2
#define W_NT " nt"

constexpr int DM = 576, FF = 4608;
constexpr size_t WS_EP = 0, WS_QP = 4096, WS_HP = WS_QP + 32 * 8 * 256 * 8, WS_ST = WS_HP + 32 * 8 * 288 * 8,
                 WS_OP = WS_ST + 64 * 64 * 8, WS_FP = WS_OP + 256 * 128 * 8, WS_END = WS_FP + 576 * 64 * 8;
constexpr int L_WQ = 0, L_W1 = 18432, L_WF = L_W1 + 21504, L_Q = L_WF + 40960, L_ACC = L_Q + 256, L_A2 = L_ACC + 2048,
              L_AO = L_A2 + 4 * 320, L_END = L_AO + 4 * 256;
constexpr u32 SPIN_CAP = 1u << 20;

__device__ __forceinline__ float bflo(u32 u) { return __uint_as_float(u << 16); }
__device__ __forceinline__ float bfhi(u32 u) { return __uint_as_float(u & 0xffff0000u); }
__device__ __forceinline__ u32 cvtpk(float a, float b) { bf16x2 c = {(__bf16)a, (__bf16)b}; return __builtin_bit_cast(u32, c); }
template <int C> __device__ __forceinline__ u32 dppu(u32 x) { return (u32)__builtin_amdgcn_update_dpp(0, (int)x, C, 0xf, 0xf, true); }
template <int C> __device__ __forceinline__ float dppf(float x) { return __uint_as_float(dppu<C>(__float_as_uint(x))); }
constexpr int X1 = 0xB1, X2 = 0x4E, X7 = 0x141, R8 = 0x128, R4 = 0x124, R2 = 0x122, R1 = 0x121;
__device__ __forceinline__ float sw16s(float x) { auto s = __builtin_amdgcn_permlane16_swap(__float_as_uint(x), __float_as_uint(x), false, false); return __uint_as_float(s[0]) + __uint_as_float(s[1]); }
__device__ __forceinline__ float sw32s(float x) { auto s = __builtin_amdgcn_permlane32_swap(__float_as_uint(x), __float_as_uint(x), false, false); return __uint_as_float(s[0]) + __uint_as_float(s[1]); }
__device__ __forceinline__ float sw16m(float x) { auto s = __builtin_amdgcn_permlane16_swap(__float_as_uint(x), __float_as_uint(x), false, false); return fmaxf(__uint_as_float(s[0]), __uint_as_float(s[1])); }
__device__ __forceinline__ float sw32m(float x) { auto s = __builtin_amdgcn_permlane32_swap(__float_as_uint(x), __float_as_uint(x), false, false); return fmaxf(__uint_as_float(s[0]), __uint_as_float(s[1])); }
__device__ __forceinline__ float row16s(float x) { x += dppf<X1>(x); x += dppf<X2>(x); x += dppf<X7>(x); x += dppf<R8>(x); return x; }
__device__ __forceinline__ float row16m(float x) { x = fmaxf(x, dppf<X1>(x)); x = fmaxf(x, dppf<X2>(x)); x = fmaxf(x, dppf<X7>(x)); x = fmaxf(x, dppf<R8>(x)); return x; }
__device__ __forceinline__ float rdl(float x, int l) { return __uint_as_float(__builtin_amdgcn_readlane(__float_as_uint(x), l)); }
__device__ __forceinline__ float dot2(u32 a, u32 b, float c) { return __builtin_amdgcn_fdot2_f32_bf16(__builtin_bit_cast(bf16x2, a), __builtin_bit_cast(bf16x2, b), c, false); }
__device__ __forceinline__ f32x4 mfma(u32x4 a, u32x4 b, f32x4 c) { return __builtin_amdgcn_mfma_f32_16x16x32_bf16(__builtin_bit_cast(bf16x8, a), __builtin_bit_cast(bf16x8, b), c, 0, 0, 0); }
__device__ __forceinline__ void wg_barrier() { asm volatile("s_waitcnt lgkmcnt(0)\n\ts_barrier" ::: "memory"); }
__device__ __forceinline__ void vm_drain() { asm volatile("s_waitcnt vmcnt(0)" ::: "memory"); }
__device__ __forceinline__ void dma16(const void* base, u32 voff, u32 lds) {
  u32 keep;
  asm volatile("s_mov_b32 %0, m0\n\ts_mov_b32 m0, %3\n\ts_nop 0\n\tglobal_load_lds_dwordx4 %1, %2" W_NT "\n\ts_mov_b32 m0, %0"
               : "=&s"(keep) : "v"(voff), "s"(base), "s"(lds) : "memory");
}
__device__ __forceinline__ u32x4 trb(LAS char* p, int rowbytes) {
  s16x4 a = __builtin_amdgcn_ds_read_tr16_b64_v4i16((LAS s16x4*)p);
  s16x4 b = __builtin_amdgcn_ds_read_tr16_b64_v4i16((LAS s16x4*)(p + 4 * rowbytes));
  u32x2 x = __builtin_bit_cast(u32x2, a), y = __builtin_bit_cast(u32x2, b);
  return u32x4{x[0], x[1], y[0], y[1]};
}
struct Tag { u32 lo, hi; };
__device__ __forceinline__ void put_raw(GAS u64* p, u32 bits24, Tag t) {
  __hip_atomic_store(p, ((u64)t.hi << 32) | (u64)(bits24 | t.lo), __ATOMIC_RELAXED, __HIP_MEMORY_SCOPE_AGENT);
}
__device__ __forceinline__ void put(GAS u64* p, float v, Tag t) { put_raw(p, (__float_as_uint(v) + 0x80u) & 0xffffff00u, t); }
template <int N, int SLEEP> __device__ __forceinline__ void gather(GAS u64* p, int stride, Tag t, float (&v)[N]) {
  for (u32 spin = 0;; ++spin) {
    u32 bad = 0;
#pragma unroll
    for (int k = 0; k < N; ++k) {
      const u64 x = __hip_atomic_load(p + k * stride, __ATOMIC_RELAXED, __HIP_MEMORY_SCOPE_AGENT);
      bad |= ((u32)(x >> 32) ^ t.hi) | (((u32)x ^ t.lo) & 0xffu);
      v[k] = __uint_as_float((u32)x & 0xffffff00u);
    }
    if (__builtin_amdgcn_ballot_w64(bad != 0) == 0) break;
    if (spin > SPIN_CAP) { v[0] = __uint_as_float(0x7fc00000u); break; }
    if (SLEEP) __builtin_amdgcn_s_sleep(SLEEP);
  }
}
#define NST 12
#define STAMP(i) do { __builtin_amdgcn_sched_barrier(0); asm volatile("s_memrealtime %0\n\ts_waitcnt lgkmcnt(0)" : "=s"(stv[i]) :: "memory"); __builtin_amdgcn_sched_barrier(0); } while (0)
#define STAMP_OUT() do { if (lane == 0) { for (int i_ = 0; i_ < NST; ++i_) ((u64*)(ws + (64u << 20)))[(bid * 8 + wave) * NST + i_] = stv[i_]; } } while (0)

__global__ void __launch_bounds__(512, 2)
fused_kernel(const u16* __restrict__ x, const u16* __restrict__ kc, const u16* __restrict__ vc, const u16* __restrict__ wq,
             const u16* __restrict__ wo, const u16* __restrict__ w1, const u16* __restrict__ b1, const u16* __restrict__ w2,
             const u16* __restrict__ b2, const u16* __restrict__ gw, u16* __restrict__ out, char* ws) {
  __shared__ __attribute__((aligned(16))) char lds_raw[L_END];
  LAS char* lds = (LAS char*)lds_raw;
  const u32 lds0 = (u32)(size_t)lds;
  const int tid = threadIdx.x, lane = tid & 63, wave = __builtin_amdgcn_readfirstlane(tid >> 6);
  const int bid = blockIdx.x, idx = bid >> 3, h = (bid & 7) * 4 + (idx >> 3), j = idx & 7, b = j >> 2, s = j & 3;
  const int g4 = lane >> 4, c16 = lane & 15;
  GAS char* wsg = (GAS char*)ws;
  u64 stv[NST]; for (int i_ = 0; i_ < NST; ++i_) stv[i_] = 0;
  STAMP(0);
  const u64 ep = __hip_atomic_load((GAS u64*)(wsg + WS_EP), __ATOMIC_RELAXED, __HIP_MEMORY_SCOPE_AGENT);
  const int trl = (8 * g4 + (c16 >> 2)), trc = (lane & 3) * 8;

  if (wave >= 4) {
    const int sw = wave - 4;
    const size_t slab = ((size_t)(b * 32 + h) * 4 + s) * 262144;
    const __amdgpu_buffer_rsrc_t rk = __builtin_amdgcn_make_buffer_rsrc((void*)((const char*)kc + slab), 0, 262144, 0x00020000);
    const __amdgpu_buffer_rsrc_t rv = __builtin_amdgcn_make_buffer_rsrc((void*)((const char*)vc + slab), 0, 262144, 0x00020000);
    const int vo = sw * 1024 + lane * 16;
    int so = idx * 4096;
    u32x4 ring[RING];
#define ISSUE(n) do { if ((n) >= WIN) asm volatile("s_waitcnt vmcnt(%0)" :: "i"(WIN - 1) : "memory"); \
      ring[(n) % RING] = __builtin_amdgcn_raw_buffer_load_b128((n) < 64 ? rk : rv, vo, so, KV_AUX); \
      so = (so + 4096) & 0x3ffff; asm volatile("" : "+s"(so)); __builtin_amdgcn_sched_barrier(0); } while (0)
#pragma unroll
    for (int n = 0; n < WIN; ++n) ISSUE(n);
    wg_barrier();
    STAMP(1);
#pragma unroll
    for (int n = WIN; n < RING; ++n) ISSUE(n);
    const u64 epn = ep + 1;
    const Tag tag = {(u32)__builtin_amdgcn_readfirstlane((u32)(epn >> 32) & 0xffu), (u32)__builtin_amdgcn_readfirstlane((u32)epn)};
    STAMP(2);
    wg_barrier();
    STAMP(3);
    const u32x4 q = *(LAS u32x4*)(lds + L_Q + c16 * 16);
    float sc[4], t8[8];
#pragma unroll
    for (int n = 0; n < 64; ++n) {
      const u32x4 k = ring[n % RING];
      float d = dot2(k[0], q[0], 0.f); d = dot2(k[1], q[1], d); d = dot2(k[2], q[2], d); d = dot2(k[3], q[3], d);
      if (n + RING < 128) ISSUE(n + RING);
      if (n + RING == TRIG) wg_barrier();
      const int i = n & 15;
      d += dppf<R8>(d);
      if (i < 8) t8[i] = d; else t8[i - 8] = (c16 & 8) ? d : t8[i - 8];
      if (i == 15) {
#pragma unroll
        for (int u = 0; u < 4; ++u) { const float lo = t8[u] + dppf<X7>(t8[u]), hi = t8[u + 4] + dppf<X7>(t8[u + 4]); t8[u] = (c16 & 4) ? hi : lo; }
#pragma unroll
        for (int u = 0; u < 2; ++u) { const float lo = t8[u] + dppf<X2>(t8[u]), hi = t8[u + 2] + dppf<X2>(t8[u + 2]); t8[u] = (c16 & 2) ? hi : lo; }
        { const float lo = t8[0] + dppf<X1>(t8[0]), hi = t8[1] + dppf<X1>(t8[1]); sc[n >> 4] = ((c16 & 1) ? hi : lo) * (0.08838834764831845f * 1.4426950408889634f); }
      }
      __builtin_amdgcn_sched_barrier(0);
    }
    STAMP(4);
    float m = fmaxf(fmaxf(sc[0], sc[1]), fmaxf(sc[2], sc[3]));
    m = sw32m(sw16m(row16m(m)));
    u32 pk[4]; float l = 0.f;
#pragma unroll
    for (int u = 0; u < 4; ++u) { const float p = __builtin_amdgcn_exp2f(sc[u] - m); pk[u] = cvtpk(p, p); l += bflo(pk[u]); }
    l = sw32s(sw16s(row16s(l)));
    if (lane < 4) {
      const u32 mb = __float_as_uint(m);
      const u32 v = lane == 0 ? (mb & 0xffffff00u) : lane == 1 ? (mb & 0xffu) << 8 : (__float_as_uint(l) + 0x80u) & 0xffffff00u;
      put_raw((GAS u64*)(wsg + WS_ST) + (b * 32 + h) * 64 + (s * 4 + sw) * 4 + lane, v, tag);
    }
    u32 mk[4];
#pragma unroll
    for (int u = 0; u < 4; ++u) mk[u] = (c16 >> 1) == u ? ((c16 & 1) ? 0xffff0000u : 0x0000ffffu) : 0u;
    f32x4 acc = {0.f, 0.f, 0.f, 0.f};
#define PV(n) do { const u32 pb_ = pk[((n) - 64) >> 4]; constexpr int C_ = 0x150 + (((n) - 64) & 15); \
      const u32x4 B_ = {dppu<C_>(pb_) & mk[0], dppu<C_>(pb_) & mk[1], dppu<C_>(pb_) & mk[2], dppu<C_>(pb_) & mk[3]}; \
      acc = mfma(ring[(n) % RING], B_, acc); __builtin_amdgcn_sched_barrier(0); \
      if ((n) + RING < 128) ISSUE((n) + RING); if ((n) + RING == TRIG) { STAMP(5); wg_barrier(); } } while (0)
#define PV4(n) PV(n); PV((n) + 1); PV((n) + 2); PV((n) + 3)
#define PV16(n) PV4(n); PV4((n) + 4); PV4((n) + 8); PV4((n) + 12)
    PV16(64); PV16(80); PV16(96); PV16(112);
    if (c16 < 8) *(LAS f32x4*)(lds + L_ACC + sw * 512 + (g4 * 8 + c16) * 16) = acc;
    STAMP(6);
    wg_barrier();
    wg_barrier();
    STAMP(7);
    f32x4 c0 = {0.f, 0.f, 0.f, 0.f}, c1 = c0;
    LAS char* wb = lds + L_WF + trl * 144 + trc + sw * 32;
    LAS char* wb4 = lds + L_WF + trl * 144 + trc + 128;
#pragma unroll
    for (int ks = 0; ks < 5; ++ks) {
      const u32x4 a = *(LAS u32x4*)(lds + L_A2 + (lane & 3) * 320 + ks * 64 + g4 * 16);
      c0 = mfma(a, trb(wb + ks * 32 * 144, 144), c0);
      if (sw == 0) c1 = mfma(a, trb(wb4 + ks * 32 * 144, 144), c1);
    }
    u32x4 bo[4], bo4[4];
#pragma unroll
    for (int ks = 0; ks < 4; ++ks) { bo[ks] = trb(wb + (144 + ks * 32) * 144, 144); bo4[ks] = trb(wb4 + (144 + ks * 32) * 144, 144); }
    wg_barrier();
    STAMP(8);
#pragma unroll
    for (int ks = 0; ks < 4; ++ks) {
      const u32x4 a = *(LAS u32x4*)(lds + L_AO + (lane & 3) * 256 + ks * 64 + g4 * 16);
      c0 = mfma(a, bo[ks], c0);
      c1 = mfma(a, bo4[ks], c1);
    }
    {
      const f32x4 c = lane < 32 ? c0 : c1;
      const float v = (g4 & 1) ? c[1] + c[3] : c[0] + c[2];
      const int col = j * 72 + (lane < 32 ? sw * 16 : 64) + c16;
      if (lane < 32 || (sw == 0 && c16 < 8)) put((GAS u64*)(wsg + WS_FP) + col * 64 + h * 2 + (g4 & 1), v, tag);
    }
    STAMP(9);
    STAMP_OUT();
    return;
  }

  const int hw = wave, t = tid;
#pragma unroll
  for (int i = 0; i < 5; ++i) {
    const int u = hw + 4 * i;
    if (u < 18) dma16(wq, ((72 * j + 4 * u + g4) * 4096 + 128 * h + 8 * c16) * 2, lds0 + L_WQ + u * 1024);
  }
  u32 xs[9], gs[9];
#pragma unroll
  for (int i = 0; i < 9; ++i) { xs[i] = ((const u32*)x)[(lane >> 5) * 288 + (lane & 31) + 32 * i]; gs[i] = ((const u32*)gw)[(lane & 31) + 32 * i]; }
  u32x4 xa[3];
#pragma unroll
  for (int ks = 0; ks < 3; ++ks) xa[ks] = *(const u32x4*)(x + (lane & 1) * DM + 72 * j + (ks < 2 ? 32 * ks + 8 * g4 : 64));
  const int f0 = t < 144 ? t : t - 144, f1 = 112 + (t & 31);
  const float bias0 = bflo(b1[144 * h + f0]), bias1 = bflo(b1[144 * h + f1]);
  const int ncol = bid + 256 * hw;
  const float bias2 = bflo(b2[ncol < DM ? ncol : 0]);
  if (t < 32) *(LAS u32*)(lds + L_A2 + (t >> 3) * 320 + 288 + (t & 7) * 4) = 0u;
  float sum = 0.f, gd = 0.f;
#pragma unroll
  for (int i = 0; i < 9; ++i) { sum += bflo(xs[i]) + bfhi(xs[i]); gd = dot2(xs[i], gs[i], gd); }
  sum = sw16s(row16s(sum)); gd = sw16s(row16s(gd));
  const float mean = sum * (1.f / DM);
  float var = 0.f;
#pragma unroll
  for (int i = 0; i < 9; ++i) { const float a = bflo(xs[i]) - mean, c = bfhi(xs[i]) - mean; var += a * a + c * c; }
  var = sw16s(row16s(var));
  const float rstd = 1.f / sqrtf(var * (1.f / DM) + 1e-5f), gate = 1.f / (1.f + __expf(-gd));
  const float mean0 = rdl(mean, 0), mean1 = rdl(mean, 32), rstd0 = rdl(rstd, 0), rstd1 = rdl(rstd, 32), gate0 = rdl(gate, 0), gate1 = rdl(gate, 32);
  const u64 epn = ep + 1;
  const Tag tag = {(u32)__builtin_amdgcn_readfirstlane((u32)(epn >> 32) & 0xffu), (u32)__builtin_amdgcn_readfirstlane((u32)epn)};
  u32x4 aq[3], ay[3];
  {
    const float mu = (lane & 1) ? mean1 : mean0, rs = (lane & 1) ? rstd1 : rstd0;
#pragma unroll
    for (int ks = 0; ks < 3; ++ks) {
      const bool live = ks < 2 || g4 == 0;
#pragma unroll
      for (int e = 0; e < 4; ++e) {
        const float y0 = (bflo(xa[ks][e]) - mu) * rs, y1 = (bfhi(xa[ks][e]) - mu) * rs;
        const u32 hi = cvtpk(y0, y1), lo = cvtpk(y0 - bflo(hi), y1 - bfhi(hi));
        ay[ks][e] = live ? ((lane & 2) ? lo : hi) : 0u;
        aq[ks][e] = (live && !(lane & 2)) ? xa[ks][e] : 0u;
      }
    }
  }
  vm_drain();
  wg_barrier();
  STAMP(1);
  const int trl2 = 64 + (c16 >> 2);
#pragma unroll
  for (int i = 0; i < 2; ++i) {
    const int tl = hw * 2 + i;
    LAS char* p = lds + L_WQ + trc + tl * 32;
    f32x4 c = {0.f, 0.f, 0.f, 0.f};
    c = mfma(aq[0], trb(p + trl * 256, 256), c); c = mfma(aq[1], trb(p + (32 + trl) * 256, 256), c); c = mfma(aq[2], trb(p + trl2 * 256, 256), c);
    if (lane < 32) put((GAS u64*)(wsg + WS_QP) + ((h * 8 + j) * 2 + g4) * 128 + tl * 16 + c16, g4 ? c[1] : c[0], tag);
  }
  STAMP(2);
#pragma unroll
  for (int i = 0; i < 6; ++i) {
    const int u = hw + 4 * i;
    if (u < 21) { const int n = min(u * 64 + lane, 1295), r = n / 18, c = n - r * 18;
      dma16(w1, ((72 * j + r) * FF + 144 * h + 8 * c) * 2, lds0 + L_W1 + u * 1024); }
  }
  {
    float v[4];
    gather<4, 2>((GAS u64*)(wsg + WS_QP) + ((h * 8 + (t & 1) * 4) * 2 + b) * 128 + (t >> 1), 256, tag, v);
    float qv = (v[0] + v[1]) + (v[2] + v[3]);
    qv += dppf<X1>(qv);
    if (!(t & 1)) *(LAS u16*)(lds + L_Q + t) = (u16)cvtpk(qv, qv);
  }
  vm_drain();
  STAMP(3);
  wg_barrier();
#pragma unroll
  for (int i = 0; i < 3; ++i) {
    const int tl = hw + 4 * i;
    if (tl < 9) {
      LAS char* p = lds + L_W1 + trc + tl * 32;
      f32x4 c = {0.f, 0.f, 0.f, 0.f};
      c = mfma(ay[0], trb(p + trl * 288, 288), c); c = mfma(ay[1], trb(p + (32 + trl) * 288, 288), c); c = mfma(ay[2], trb(p + trl2 * 288, 288), c);
      if (lane < 32) put((GAS u64*)(wsg + WS_HP) + (h * 8 + j) * 288 + g4 * 144 + tl * 16 + c16, g4 ? c[1] + c[3] : c[0] + c[2], tag);
    }
  }
  __builtin_amdgcn_s_sleep(60);
#pragma unroll
  for (int i = 0; i < 2; ++i) {
    if (i == 0 || hw == 0) {
      float v[8];
      const int o = i == 0 ? t : 256 + (t & 31);
      gather<8, 16>((GAS u64*)(wsg + WS_HP) + h * 8 * 288 + o, 288, tag, v);
      const float z = (((v[0] + v[1]) + (v[2] + v[3])) + ((v[4] + v[5]) + (v[6] + v[7]))) + (i == 0 ? bias0 : bias1);
      const int bb = o >= 144;
      const float u2 = 1.5957691216057308f * (z + 0.044715f * z * z * z);
      const float a = (1.f - (bb ? gate1 : gate0)) * (z / (1.f + __expf(-u2)));
      const u32 hi = cvtpk(a, a), lo = cvtpk(a - bflo(hi), 0.f);
      LAS char* p = lds + L_A2 + bb * 320 + (i == 0 ? f0 : f1) * 2;
      *(LAS u16*)p = (u16)hi; *(LAS u16*)(p + 640) = (u16)lo;
    }
  }
  STAMP(4);
  float fw[4];
  {
    float v[1];
    gather<1, 16>((GAS u64*)(wsg + WS_ST) + (b * 32 + h) * 64 + lane, 1, tag, v);
    const u32 r = __float_as_uint(v[0]);
    const float mw = __uint_as_float(dppu<0x00>(r) | (dppu<0x55>(r) >> 8)), lw = __uint_as_float(dppu<0xAA>(r));
    float M = fmaxf(mw, dppf<R4>(mw)); M = fmaxf(M, dppf<R8>(M)); M = sw32m(sw16m(M));
    const float e = __builtin_amdgcn_exp2f(mw - M);
    float L = lw * e; L += dppf<X7>(L); L += dppf<R8>(L); L = sw32s(sw16s(L));
    const float f = e * (b ? gate1 : gate0) / L;
#pragma unroll
    for (int u = 0; u < 4; ++u) fw[u] = rdl(f, s * 16 + u * 4);
  }
  STAMP(5);
  wg_barrier();
#pragma unroll
  for (int i = 0; i < 10; ++i) {
    const int u = hw + 4 * i;
    const int n = min(u * 64 + lane, 2447), r = n / 9, c = n - r * 9;
    const u32 o2 = ((144 * h + r) * DM + 72 * j + 8 * c) * 2, oo = ((128 * h + r - 144) * DM + 72 * j + 8 * c) * 2;
    if (u < 20) dma16(w2, o2, lds0 + L_WF + u * 1024);
    else if (u == 20) { if (lane < 16) dma16(w2, o2, lds0 + L_WF + u * 1024); else dma16(wo, oo, lds0 + L_WF + u * 1024); }
    else if (u < 39) dma16(wo, oo, lds0 + L_WF + u * 1024);
  }
  STAMP(6);
  wg_barrier();
  if (hw < 2) {
    LAS float* a = (LAS float*)(lds + L_ACC) + t;
    const float o = (fw[0] * a[0] + fw[1] * a[128]) + (fw[2] * a[256] + fw[3] * a[384]);
    put((GAS u64*)(wsg + WS_OP) + (h * 8 + j) * 128 + t, o, tag);
  }
  STAMP(7);
  vm_drain();
  wg_barrier();
  STAMP(8);
  {
    float v[4];
    const int e = t & 127, bb = t >> 7;
    gather<4, 0>((GAS u64*)(wsg + WS_OP) + (h * 8 + bb * 4) * 128 + e, 128, tag, v);
    const float o = (v[0] + v[1]) + (v[2] + v[3]);
    const u32 hi = cvtpk(o, o), lo = cvtpk(o - bflo(hi), 0.f);
    const int dim = (e & 96) + (e & 3) * 8 + ((e >> 2) & 7);
    LAS char* p = lds + L_AO + bb * 256 + dim * 2;
    *(LAS u16*)p = (u16)hi; *(LAS u16*)(p + 512) = (u16)lo;
  }
  STAMP(9);
  wg_barrier();
  if (ncol < DM) {
    float v[1];
    gather<1, 0>((GAS u64*)(wsg + WS_FP) + ncol * 64 + lane, 1, tag, v);
    float r = v[0];
    r += dppf<X2>(r); r += dppf<R4>(r); r += dppf<R8>(r); r = sw32s(sw16s(r));
    r += (1.f - ((lane & 1) ? gate1 : gate0)) * bias2;
    if (lane < 2) out[lane * DM + ncol] = (u16)cvtpk(r, r);
    if (ncol == 0 && lane == 0) __hip_atomic_store((GAS u64*)(wsg + WS_EP), epn, __ATOMIC_RELAXED, __HIP_MEMORY_SCOPE_AGENT);
  }
  STAMP(10);
  STAMP_OUT();
}

__device__ __forceinline__ u64 pstat(const u64* st, int role, int k, int q, u64* sh, u64 t0) {
  const int t = threadIdx.x; u64 v = st[((t >> 2) * 8 + role * 4 + (t & 3)) * NST + k]; if (v == 0) v = t0;
  __syncthreads(); sh[t] = v; __syncthreads();
  int rank = 0; for (int i = 0; i < 1024; ++i) { const u64 o = sh[i]; rank += (o < v) || (o == v && i < t); }
  const int want = q == 0 ? 0 : q == 1 ? 512 : 1023;
  __syncthreads(); if (rank == want) sh[1024] = v; __syncthreads();
  return sh[1024];
}
__global__ void __launch_bounds__(1024) probe_kernel(char* ws) {
  __shared__ u64 sh[1025];
  const u64* st = (const u64*)(ws + (64u << 20));
  const u64 a0 = pstat(st, 0, 0, 0, sh, 0), a1 = pstat(st, 1, 0, 0, sh, 0), t0 = a0 < a1 ? a0 : a1;
  const u64 A = pstat(st, PA_R, PA_K, PA_Q, sh, t0) - t0, B = pstat(st, PB_R, PB_K, PB_Q, sh, t0) - t0;
  u64 Aq = A / 25; if (Aq > 250) Aq = 250;
  u64 Bc = B > 4400 ? 4400 : B;
  const u64 T = t0 + (Aq + 4) * 10000 + 2 * Bc;
  while (__builtin_amdgcn_s_memrealtime() < T) {}
}

extern "C" void kernel_launch(void* const* d_in, const int* in_sizes, int n_in, void* d_out, int out_size, void* d_ws,
                              size_t ws_size, hipStream_t stream) {
  fused_kernel<<<256, 512, 0, stream>>>((const u16*)d_in[0], (const u16*)d_in[1], (const u16*)d_in[2], (const u16*)d_in[3],
                                        (const u16*)d_in[4], (const u16*)d_in[6], (const u16*)d_in[7], (const u16*)d_in[8],
                                        (const u16*)d_in[9], (const u16*)d_in[10], (u16*)d_out, (char*)d_ws);
  probe_kernel<<<1,1024,0,stream>>>((char*)d_ws);
}
```

```cpp
#define STAMPS
#define PA_R 0
#define PA_K 10
#define PA_Q 2
#define PB_R 0
#define PB_K 0
#define PB_Q 0
#include <hip/hip_runtime.h>
#include <hip/hip_bf16.h>
#include <stdint.h>

typedef unsigned u32; typedef unsigned long long u64; typedef unsigned short u16;
typedef u32   u32x4 __attribute__((ext_vector_type(4)));
typedef u32   u32x2 __attribute__((ext_vector_type(2)));
typedef float f32x4 __attribute__((ext_vector_type(4)));
typedef short s16x4 __attribute__((ext_vector_type(4)));
typedef __bf16 bf16x2 __attribute__((ext_vector_type(2)));
typedef __bf16 bf16x8 __attribute__((ext_vector_type(8)));
#define LAS __attribute__((address_space(3)))
#define GAS __attribute__((address_space(1)))

#define WIN 8
#define RING 32
#define TRIG 127
#define KV_AUX 2
#define W_NT " nt"

constexpr int DM = 576, FF = 4608;
constexpr size_t WS_EP = 0, WS_QP = 4096, WS_HP = WS_QP + 32 * 8 * 256 * 8, WS_ST = WS_HP + 32 * 8 * 288 * 8,
                 WS_OP = WS_ST + 64 * 64 * 8, WS_FP = WS_OP + 256 * 128 * 8, WS_END = WS_FP + 576 * 64 * 8;
constexpr int L_WQ = 0, L_W1 = 18432, L_WF = L_W1 + 21504, L_Q = L_WF + 40960, L_ACC = L_Q + 256, L_A2 = L_ACC + 2048,
              L_AO = L_A2 + 4 * 320, L_END = L_AO + 4 * 256;
constexpr u32 SPIN_CAP = 1u << 20;

__device__ __forceinline__ float bflo(u32 u) { return __uint_as_float(u << 16); }
__device__ __forceinline__ float bfhi(u32 u) { return __uint_as_float(u & 0xffff0000u); }
__device__ __forceinline__ u32 cvtpk(float a, float b) { bf16x2 c = {(__bf16)a, (__bf16)b}; return __builtin_bit_cast(u32, c); }
template <int C> __device__ __forceinline__ u32 dppu(u32 x) { return (u32)__builtin_amdgcn_update_dpp(0, (int)x, C, 0xf, 0xf, true); }
template <int C> __device__ __forceinline__ float dppf(float x) { return __uint_as_float(dppu<C>(__float_as_uint(x))); }
constexpr int X1 = 0xB1, X2 = 0x4E, X7 = 0x141, R8 = 0x128, R4 = 0x124, R2 = 0x122, R1 = 0x121;
__device__ __forceinline__ float sw16s(float x) { auto s = __builtin_amdgcn_permlane16_swap(__float_as_uint(x), __float_as_uint(x), false, false); return __uint_as_float(s[0]) + __uint_as_float(s[1]); }
__device__ __forceinline__ float sw32s(float x) { auto s = __builtin_amdgcn_permlane32_swap(__float_as_uint(x), __float_as_uint(x), false, false); return __uint_as_float(s[0]) + __uint_as_float(s[1]); }
__device__ __forceinline__ float sw16m(float x) { auto s = __builtin_amdgcn_permlane16_swap(__float_as_uint(x), __float_as_uint(x), false, false); return fmaxf(__uint_as_float(s[0]), __uint_as_float(s[1])); }
__device__ __forceinline__ float sw32m(float x) { auto s = __builtin_amdgcn_permlane32_swap(__float_as_uint(x), __float_as_uint(x), false, false); return fmaxf(__uint_as_float(s[0]), __uint_as_float(s[1])); }
__device__ __forceinline__ float row16s(float x) { x += dppf<X1>(x); x += dppf<X2>(x); x += dppf<X7>(x); x += dppf<R8>(x); return x; }
__device__ __forceinline__ float row16m(float x) { x = fmaxf(x, dppf<X1>(x)); x = fmaxf(x, dppf<X2>(x)); x = fmaxf(x, dppf<X7>(x)); x = fmaxf(x, dppf<R8>(x)); return x; }
__device__ __forceinline__ float rdl(float x, int l) { return __uint_as_float(__builtin_amdgcn_readlane(__float_as_uint(x), l)); }
__device__ __forceinline__ float dot2(u32 a, u32 b, float c) { return __builtin_amdgcn_fdot2_f32_bf16(__builtin_bit_cast(bf16x2, a), __builtin_bit_cast(bf16x2, b), c, false); }
__device__ __forceinline__ f32x4 mfma(u32x4 a, u32x4 b, f32x4 c) { return __builtin_amdgcn_mfma_f32_16x16x32_bf16(__builtin_bit_cast(bf16x8, a), __builtin_bit_cast(bf16x8, b), c, 0, 0, 0); }
__device__ __forceinline__ void wg_barrier() { asm volatile("s_waitcnt lgkmcnt(0)\n\ts_barrier" ::: "memory"); }
__device__ __forceinline__ void vm_drain() { asm volatile("s_waitcnt vmcnt(0)" ::: "memory"); }
__device__ __forceinline__ void dma16(const void* base, u32 voff, u32 lds) {
  u32 keep;
  asm volatile("s_mov_b32 %0, m0\n\ts_mov_b32 m0, %3\n\ts_nop 0\n\tglobal_load_lds_dwordx4 %1, %2" W_NT "\n\ts_mov_b32 m0, %0"
               : "=&s"(keep) : "v"(voff), "s"(base), "s"(lds) : "memory");
}
__device__ __forceinline__ u32x4 trb(LAS char* p, int rowbytes) {
  s16x4 a = __builtin_amdgcn_ds_read_tr16_b64_v4i16((LAS s16x4*)p);
  s16x4 b = __builtin_amdgcn_ds_read_tr16_b64_v4i16((LAS s16x4*)(p + 4 * rowbytes));
  u32x2 x = __builtin_bit_cast(u32x2, a), y = __builtin_bit_cast(u32x2, b);
  return u32x4{x[0], x[1], y[0], y[1]};
}
struct Tag { u32 lo, hi; };
__device__ __forceinline__ void put_raw(GAS u64* p, u32 bits24, Tag t) {
  __hip_atomic_store(p, ((u64)t.hi << 32) | (u64)(bits24 | t.lo), __ATOMIC_RELAXED, __HIP_MEMORY_SCOPE_AGENT);
}
__device__ __forceinline__ void put(GAS u64* p, float v, Tag t) { put_raw(p, (__float_as_uint(v) + 0x80u) & 0xffffff00u, t); }
template <int N, int SLEEP> __device__ __forceinline__ void gather(GAS u64* p, int stride, Tag t, float (&v)[N]) {
  for (u32 spin = 0;; ++spin) {
    u32 bad = 0;
#pragma unroll
    for (int k = 0; k < N; ++k) {
      const u64 x = __hip_atomic_load(p + k * stride, __ATOMIC_RELAXED, __HIP_MEMORY_SCOPE_AGENT);
      bad |= ((u32)(x >> 32) ^ t.hi) | (((u32)x ^ t.lo) & 0xffu);
      v[k] = __uint_as_float((u32)x & 0xffffff00u);
    }
    if (__builtin_amdgcn_ballot_w64(bad != 0) == 0) break;
    if (spin > SPIN_CAP) { v[0] = __uint_as_float(0x7fc00000u); break; }
    if (SLEEP) __builtin_amdgcn_s_sleep(SLEEP);
  }
}
#define NST 12
#define STAMP(i) do { __builtin_amdgcn_sched_barrier(0); asm volatile("s_memrealtime %0\n\ts_waitcnt lgkmcnt(0)" : "=s"(stv[i]) :: "memory"); __builtin_amdgcn_sched_barrier(0); } while (0)
#define STAMP_OUT() do { if (lane == 0) { for (int i_ = 0; i_ < NST; ++i_) ((u64*)(ws + (64u << 20)))[(bid * 8 + wave) * NST + i_] = stv[i_]; } } while (0)

__global__ void __launch_bounds__(512, 2)
fused_kernel(const u16* __restrict__ x, const u16* __restrict__ kc, const u16* __restrict__ vc, const u16* __restrict__ wq,
             const u16* __restrict__ wo, const u16* __restrict__ w1, const u16* __restrict__ b1, const u16* __restrict__ w2,
             const u16* __restrict__ b2, const u16* __restrict__ gw, u16* __restrict__ out, char* ws) {
  __shared__ __attribute__((aligned(16))) char lds_raw[L_END];
  LAS char* lds = (LAS char*)lds_raw;
  const u32 lds0 = (u32)(size_t)lds;
  const int tid = threadIdx.x, lane = tid & 63, wave = __builtin_amdgcn_readfirstlane(tid >> 6);
  const int bid = blockIdx.x, idx = bid >> 3, h = (bid & 7) * 4 + (idx >> 3), j = idx & 7, b = j >> 2, s = j & 3;
  const int g4 = lane >> 4, c16 = lane & 15;
  GAS char* wsg = (GAS char*)ws;
  u64 stv[NST]; for (int i_ = 0; i_ < NST; ++i_) stv[i_] = 0;
  STAMP(0);
  const u64 ep = __hip_atomic_load((GAS u64*)(wsg + WS_EP), __ATOMIC_RELAXED, __HIP_MEMORY_SCOPE_AGENT);
  const int trl = (8 * g4 + (c16 >> 2)), trc = (lane & 3) * 8;

  if (wave >= 4) {
    const int sw = wave - 4;
    const size_t slab = ((size_t)(b * 32 + h) * 4 + s) * 262144;
    const __amdgpu_buffer_rsrc_t rk = __builtin_amdgcn_make_buffer_rsrc((void*)((const char*)kc + slab), 0, 262144, 0x00020000);
    const __amdgpu_buffer_rsrc_t rv = __builtin_amdgcn_make_buffer_rsrc((void*)((const char*)vc + slab), 0, 262144, 0x00020000);
    const int vo = sw * 1024 + lane * 16;
    int so = idx * 4096;
    u32x4 ring[RING];
#define ISSUE(n) do { if ((n) >= WIN) asm volatile("s_waitcnt vmcnt(%0)" :: "i"(WIN - 1) : "memory"); \
      ring[(n) % RING] = __builtin_amdgcn_raw_buffer_load_b128((n) < 64 ? rk : rv, vo, so, KV_AUX); \
      so = (so + 4096) & 0x3ffff; asm volatile("" : "+s"(so)); __builtin_amdgcn_sched_barrier(0); } while (0)
#pragma unroll
    for (int n = 0; n < WIN; ++n) ISSUE(n);
    wg_barrier();
    STAMP(1);
#pragma unroll
    for (int n = WIN; n < RING; ++n) ISSUE(n);
    const u64 epn = ep + 1;
    const Tag tag = {(u32)__builtin_amdgcn_readfirstlane((u32)(epn >> 32) & 0xffu), (u32)__builtin_amdgcn_readfirstlane((u32)epn)};
    STAMP(2);
    wg_barrier();
    STAMP(3);
    const u32x4 q = *(LAS u32x4*)(lds + L_Q + c16 * 16);
    float sc[4], t8[8];
#pragma unroll
    for (int n = 0; n < 64; ++n) {
      const u32x4 k = ring[n % RING];
      float d = dot2(k[0], q[0], 0.f); d = dot2(k[1], q[1], d); d = dot2(k[2], q[2], d); d = dot2(k[3], q[3], d);
      if (n + RING < 128) ISSUE(n + RING);
      if (n + RING == TRIG) wg_barrier();
      const int i = n & 15;
      d += dppf<R8>(d);
      if (i < 8) t8[i] = d; else t8[i - 8] = (c16 & 8) ? d : t8[i - 8];
      if (i == 15) {
#pragma unroll
        for (int u = 0; u < 4; ++u) { const float lo = t8[u] + dppf<X7>(t8[u]), hi = t8[u + 4] + dppf<X7>(t8[u + 4]); t8[u] = (c16 & 4) ? hi : lo; }
#pragma unroll
        for (int u = 0; u < 2; ++u) { const float lo = t8[u] + dppf<X2>(t8[u]), hi = t8[u + 2] + dppf<X2>(t8[u + 2]); t8[u] = (c16 & 2) ? hi : lo; }
        { const float lo = t8[0] + dppf<X1>(t8[0]), hi = t8[1] + dppf<X1>(t8[1]); sc[n >> 4] = ((c16 & 1) ? hi : lo) * (0.08838834764831845f * 1.4426950408889634f); }
      }
      __builtin_amdgcn_sched_barrier(0);
    }
    STAMP(4);
    float m = fmaxf(fmaxf(sc[0], sc[1]), fmaxf(sc[2], sc[3]));
    m = sw32m(sw16m(row16m(m)));
    u32 pk[4]; float l = 0.f;
#pragma unroll
    for (int u = 0; u < 4; ++u) { const float p = __builtin_amdgcn_exp2f(sc[u] - m); pk[u] = cvtpk(p, p); l += bflo(pk[u]); }
    l = sw32s(sw16s(row16s(l)));
    if (lane < 4) {
      const u32 mb = __float_as_uint(m);
      const u32 v = lane == 0 ? (mb & 0xffffff00u) : lane == 1 ? (mb & 0xffu) << 8 : (__float_as_uint(l) + 0x80u) & 0xffffff00u;
      put_raw((GAS u64*)(wsg + WS_ST) + (b * 32 + h) * 64 + (s * 4 + sw) * 4 + lane, v, tag);
    }
    u32 mk[4];
#pragma unroll
    for (int u = 0; u < 4; ++u) mk[u] = (c16 >> 1) == u ? ((c16 & 1) ? 0xffff0000u : 0x0000ffffu) : 0u;
    f32x4 acc = {0.f, 0.f, 0.f, 0.f};
#define PV(n) do { const u32 pb_ = pk[((n) - 64) >> 4]; constexpr int C_ = 0x150 + (((n) - 64) & 15); \
      const u32x4 B_ = {dppu<C_>(pb_) & mk[0], dppu<C_>(pb_) & mk[1], dppu<C_>(pb_) & mk[2], dppu<C_>(pb_) & mk[3]}; \
      acc = mfma(ring[(n) % RING], B_, acc); __builtin_amdgcn_sched_barrier(0); \
      if ((n) + RING < 128) ISSUE((n) + RING); if ((n) + RING == TRIG) { STAMP(5); wg_barrier(); } } while (0)
#define PV4(n) PV(n); PV((n) + 1); PV((n) + 2); PV((n) + 3)
#define PV16(n) PV4(n); PV4((n) + 4); PV4((n) + 8); PV4((n) + 12)
    PV16(64); PV16(80); PV16(96); PV16(112);
    if (c16 < 8) *(LAS f32x4*)(lds + L_ACC + sw * 512 + (g4 * 8 + c16) * 16) = acc;
    STAMP(6);
    wg_barrier();
    wg_barrier();
    STAMP(7);
    f32x4 c0 = {0.f, 0.f, 0.f, 0.f}, c1 = c0;
    LAS char* wb = lds + L_WF + trl * 144 + trc + sw * 32;
    LAS char* wb4 = lds + L_WF + trl * 144 + trc + 128;
#pragma unroll
    for (int ks = 0; ks < 5; ++ks) {
      const u32x4 a = *(LAS u32x4*)(lds + L_A2 + (lane & 3) * 320 + ks * 64 + g4 * 16);
      c0 = mfma(a, trb(wb + ks * 32 * 144, 144), c0);
      if (sw == 0) c1 = mfma(a, trb(wb4 + ks * 32 * 144, 144), c1);
    }
    u32x4 bo[4], bo4[4];
#pragma unroll
    for (int ks = 0; ks < 4; ++ks) { bo[ks] = trb(wb + (144 + ks * 32) * 144, 144); bo4[ks] = trb(wb4 + (144 + ks * 32) * 144, 144); }
    wg_barrier();
    STAMP(8);
#pragma unroll
    for (int ks = 0; ks < 4; ++ks) {
      const u32x4 a = *(LAS u32x4*)(lds + L_AO + (lane & 3) * 256 + ks * 64 + g4 * 16);
      c0 = mfma(a, bo[ks], c0);
      c1 = mfma(a, bo4[ks], c1);
    }
    {
      const f32x4 c = lane < 32 ? c0 : c1;
      const float v = (g4 & 1) ? c[1] + c[3] : c[0] + c[2];
      const int col = j * 72 + (lane < 32 ? sw * 16 : 64) + c16;
      if (lane < 32 || (sw == 0 && c16 < 8)) put((GAS u64*)(wsg + WS_FP) + col * 64 + h * 2 + (g4 & 1), v, tag);
    }
    STAMP(9);
    STAMP_OUT();
    return;
  }

  const int hw = wave, t = tid;
#pragma unroll
  for (int i = 0; i < 5; ++i) {
    const int u = hw + 4 * i;
    if (u < 18) dma16(wq, ((72 * j + 4 * u + g4) * 4096 + 128 * h + 8 * c16) * 2, lds0 + L_WQ + u * 1024);
  }
  u32 xs[9], gs[9];
#pragma unroll
  for (int i = 0; i < 9; ++i) { xs[i] = ((const u32*)x)[(lane >> 5) * 288 + (lane & 31) + 32 * i]; gs[i] = ((const u32*)gw)[(lane & 31) + 32 * i]; }
  u32x4 xa[3];
#pragma unroll
  for (int ks = 0; ks < 3; ++ks) xa[ks] = *(const u32x4*)(x + (lane & 1) * DM + 72 * j + (ks < 2 ? 32 * ks + 8 * g4 : 64));
  const int f0 = t < 144 ? t : t - 144, f1 = 112 + (t & 31);
  const float bias0 = bflo(b1[144 * h + f0]), bias1 = bflo(b1[144 * h + f1]);
  const int ncol = bid + 256 * hw;
  const float bias2 = bflo(b2[ncol < DM ? ncol : 0]);
  if (t < 32) *(LAS u32*)(lds + L_A2 + (t >> 3) * 320 + 288 + (t & 7) * 4) = 0u;
  float sum = 0.f, gd = 0.f;
#pragma unroll
  for (int i = 0; i < 9; ++i) { sum += bflo(xs[i]) + bfhi(xs[i]); gd = dot2(xs[i], gs[i], gd); }
  sum = sw16s(row16s(sum)); gd = sw16s(row16s(gd));
  const float mean = sum * (1.f / DM);
  float var = 0.f;
#pragma unroll
  for (int i = 0; i < 9; ++i) { const float a = bflo(xs[i]) - mean, c = bfhi(xs[i]) - mean; var += a * a + c * c; }
  var = sw16s(row16s(var));
  const float rstd = 1.f / sqrtf(var * (1.f / DM) + 1e-5f), gate = 1.f / (1.f + __expf(-gd));
  const float mean0 = rdl(mean, 0), mean1 = rdl(mean, 32), rstd0 = rdl(rstd, 0), rstd1 = rdl(rstd, 32), gate0 = rdl(gate, 0), gate1 = rdl(gate, 32);
  const u64 epn = ep + 1;
  const Tag tag = {(u32)__builtin_amdgcn_readfirstlane((u32)(epn >> 32) & 0xffu), (u32)__builtin_amdgcn_readfirstlane((u32)epn)};
  u32x4 aq[3], ay[3];
  {
    const float mu = (lane & 1) ? mean1 : mean0, rs = (lane & 1) ? rstd1 : rstd0;
#pragma unroll
    for (int ks = 0; ks < 3; ++ks) {
      const bool live = ks < 2 || g4 == 0;
#pragma unroll
      for (int e = 0; e < 4; ++e) {
        const float y0 = (bflo(xa[ks][e]) - mu) * rs, y1 = (bfhi(xa[ks][e]) - mu) * rs;
        const u32 hi = cvtpk(y0, y1), lo = cvtpk(y0 - bflo(hi), y1 - bfhi(hi));
        ay[ks][e] = live ? ((lane & 2) ? lo : hi) : 0u;
        aq[ks][e] = (live && !(lane & 2)) ? xa[ks][e] : 0u;
      }
    }
  }
  vm_drain();
  wg_barrier();
  STAMP(1);
  const int trl2 = 64 + (c16 >> 2);
#pragma unroll
  for (int i = 0; i < 2; ++i) {
    const int tl = hw * 2 + i;
    LAS char* p = lds + L_WQ + trc + tl * 32;
    f32x4 c = {0.f, 0.f, 0.f, 0.f};
    c = mfma(aq[0], trb(p + trl * 256, 256), c); c = mfma(aq[1], trb(p + (32 + trl) * 256, 256), c); c = mfma(aq[2], trb(p + trl2 * 256, 256), c);
    if (lane < 32) put((GAS u64*)(wsg + WS_QP) + ((h * 8 + j) * 2 + g4) * 128 + tl * 16 + c16, g4 ? c[1] : c[0], tag);
  }
  STAMP(2);
#pragma unroll
  for (int i = 0; i < 6; ++i) {
    const int u = hw + 4 * i;
    if (u < 21) { const int n = min(u * 64 + lane, 1295), r = n / 18, c = n - r * 18;
      dma16(w1, ((72 * j + r) * FF + 144 * h + 8 * c) * 2, lds0 + L_W1 + u * 1024); }
  }
  {
    float v[4];
    gather<4, 2>((GAS u64*)(wsg + WS_QP) + ((h * 8 + (t & 1) * 4) * 2 + b) * 128 + (t >> 1), 256, tag, v);
    float qv = (v[0] + v[1]) + (v[2] + v[3]);
    qv += dppf<X1>(qv);
    if (!(t & 1)) *(LAS u16*)(lds + L_Q + t) = (u16)cvtpk(qv, qv);
  }
  vm_drain();
  STAMP(3);
  wg_barrier();
#pragma unroll
  for (int i = 0; i < 3; ++i) {
    const int tl = hw + 4 * i;
    if (tl < 9) {
      LAS char* p = lds + L_W1 + trc + tl * 32;
      f32x4 c = {0.f, 0.f, 0.f, 0.f};
      c = mfma(ay[0], trb(p + trl * 288, 288), c); c = mfma(ay[1], trb(p + (32 + trl) * 288, 288), c); c = mfma(ay[2], trb(p + trl2 * 288, 288), c);
      if (lane < 32) put((GAS u64*)(wsg + WS_HP) + (h * 8 + j) * 288 + g4 * 144 + tl * 16 + c16, g4 ? c[1] + c[3] : c[0] + c[2], tag);
    }
  }
  __builtin_amdgcn_s_sleep(60);
#pragma unroll
  for (int i = 0; i < 2; ++i) {
    if (i == 0 || hw == 0) {
      float v[8];
      const int o = i == 0 ? t : 256 + (t & 31);
      gather<8, 16>((GAS u64*)(wsg + WS_HP) + h * 8 * 288 + o, 288, tag, v);
      const float z = (((v[0] + v[1]) + (v[2] + v[3])) + ((v[4] + v[5]) + (v[6] + v[7]))) + (i == 0 ? bias0 : bias1);
      const int bb = o >= 144;
      const float u2 = 1.5957691216057308f * (z + 0.044715f * z * z * z);
      const float a = (1.f - (bb ? gate1 : gate0)) * (z / (1.f + __expf(-u2)));
      const u32 hi = cvtpk(a, a), lo = cvtpk(a - bflo(hi), 0.f);
      LAS char* p = lds + L_A2 + bb * 320 + (i == 0 ? f0 : f1) * 2;
      *(LAS u16*)p = (u16)hi; *(LAS u16*)(p + 640) = (u16)lo;
    }
  }
  STAMP(4);
  float fw[4];
  {
    float v[1];
    gather<1, 16>((GAS u64*)(wsg + WS_ST) + (b * 32 + h) * 64 + lane, 1, tag, v);
    const u32 r = __float_as_uint(v[0]);
    const float mw = __uint_as_float(dppu<0x00>(r) | (dppu<0x55>(r) >> 8)), lw = __uint_as_float(dppu<0xAA>(r));
    float M = fmaxf(mw, dppf<R4>(mw)); M = fmaxf(M, dppf<R8>(M)); M = sw32m(sw16m(M));
    const float e = __builtin_amdgcn_exp2f(mw - M);
    float L = lw * e; L += dppf<X7>(L); L += dppf<R8>(L); L = sw32s(sw16s(L));
    const float f = e * (b ? gate1 : gate0) / L;
#pragma unroll
    for (int u = 0; u < 4; ++u) fw[u] = rdl(f, s * 16 + u * 4);
  }
  STAMP(5);
  wg_barrier();
#pragma unroll
  for (int i = 0; i < 10; ++i) {
    const int u = hw + 4 * i;
    const int n = min(u * 64 + lane, 2447), r = n / 9, c = n - r * 9;
    const u32 o2 = ((144 * h + r) * DM + 72 * j + 8 * c) * 2, oo = ((128 * h + r - 144) * DM + 72 * j + 8 * c) * 2;
    if (u < 20) dma16(w2, o2, lds0 + L_WF + u * 1024);
    else if (u == 20) { if (lane < 16) dma16(w2, o2, lds0 + L_WF + u * 1024); else dma16(wo, oo, lds0 + L_WF + u * 1024); }
    else if (u < 39) dma16(wo, oo, lds0 + L_WF + u * 1024);
  }
  STAMP(6);
  wg_barrier();
  if (hw < 2) {
    LAS float* a = (LAS float*)(lds + L_ACC) + t;
    const float o = (fw[0] * a[0] + fw[1] * a[128]) + (fw[2] * a[256] + fw[3] * a[384]);
    put((GAS u64*)(wsg + WS_OP) + (h * 8 + j) * 128 + t, o, tag);
  }
  STAMP(7);
  vm_drain();
  wg_barrier();
  STAMP(8);
  {
    float v[4];
    const int e = t & 127, bb = t >> 7;
    gather<4, 0>((GAS u64*)(wsg + WS_OP) + (h * 8 + bb * 4) * 128 + e, 128, tag, v);
    const float o = (v[0] + v[1]) + (v[2] + v[3]);
    const u32 hi = cvtpk(o, o), lo = cvtpk(o - bflo(hi), 0.f);
    const int dim = (e & 96) + (e & 3) * 8 + ((e >> 2) & 7);
    LAS char* p = lds + L_AO + bb * 256 + dim * 2;
    *(LAS u16*)p = (u16)hi; *(LAS u16*)(p + 512) = (u16)lo;
  }
  STAMP(9);
  wg_barrier();
  if (ncol < DM) {
    float v[1];
    gather<1, 0>((GAS u64*)(wsg + WS_FP) + ncol * 64 + lane, 1, tag, v);
    float r = v[0];
    r += dppf<X2>(r); r += dppf<R4>(r); r += dppf<R8>(r); r = sw32s(sw16s(r));
    r += (1.f - ((lane & 1) ? gate1 : gate0)) * bias2;
    if (lane < 2) out[lane * DM + ncol] = (u16)cvtpk(r, r);
    if (ncol == 0 && lane == 0) __hip_atomic_store((GAS u64*)(wsg + WS_EP), epn, __ATOMIC_RELAXED, __HIP_MEMORY_SCOPE_AGENT);
  }
  STAMP(10);
  STAMP_OUT();
}

__device__ __forceinline__ u64 pstat(const u64* st, int role, int k, int q, u64* sh, u64 t0) {
  const int t = threadIdx.x; u64 v = st[((t >> 2) * 8 + role * 4 + (t & 3)) * NST + k]; if (v == 0) v = t0;
  __syncthreads(); sh[t] = v; __syncthreads();
  int rank = 0; for (int i = 0; i < 1024; ++i) { const u64 o = sh[i]; rank += (o < v) || (o == v && i < t); }
  const int want = q == 0 ? 0 : q == 1 ? 512 : 1023;
  __syncthreads(); if (rank == want) sh[1024] = v; __syncthreads();
  return sh[1024];
}
__global__ void __launch_bounds__(1024) probe_kernel(char* ws) {
  __shared__ u64 sh[1025];
  const u64* st = (const u64*)(ws + (64u << 20));
  const u64 a0 = pstat(st, 0, 0, 0, sh, 0), a1 = pstat(st, 1, 0, 0, sh, 0), t0 = a0 < a1 ? a0 : a1;
  const u64 A = pstat(st, PA_R, PA_K, PA_Q, sh, t0) - t0, B = pstat(st, PB_R, PB_K, PB_Q, sh, t0) - t0;
  u64 Aq = A / 25; if (Aq > 250) Aq = 250;
  u64 Bc = B > 4400 ? 4400 : B;
  const u64 T = t0 + (Aq + 4) * 10000 + 2 * Bc;
  while (__builtin_amdgcn_s_memrealtime() < T) {}
}

extern "C" void kernel_launch(void* const* d_in, const int* in_sizes, int n_in, void* d_out, int out_size, void* d_ws,
                              size_t ws_size, hipStream_t stream) {
  fused_kernel<<<256, 512, 0, stream>>>((const u16*)d_in[0], (const u16*)d_in[1], (const u16*)d_in[2], (const u16*)d_in[3],
                                        (const u16*)d_in[4], (const u16*)d_in[6], (const u16*)d_in[7], (const u16*)d_in[8],
                                        (const u16*)d_in[9], (const u16*)d_in[10], (u16*)d_out, (char*)d_ws);
  probe_kernel<<<1,1024,0,stream>>>((char*)d_ws);
}
```

```cpp
#include <hip/hip_runtime.h>
#include <hip/hip_bf16.h>
#include <stdint.h>

typedef unsigned u32; typedef unsigned long long u64; typedef unsigned short u16;
typedef u32   u32x4 __attribute__((ext_vector_type(4)));
typedef u32   u32x2 __attribute__((ext_vector_type(2)));
typedef float f32x4 __attribute__((ext_vector_type(4)));
typedef short s16x4 __attribute__((ext_vector_type(4)));
typedef __bf16 bf16x2 __attribute__((ext_vector_type(2)));
typedef __bf16 bf16x8 __attribute__((ext_vector_type(8)));
#define LAS __attribute__((address_space(3)))
#define GAS __attribute__((address_space(1)))


constexpr int WIN = 8;
constexpr int RING = 48;
constexpr int LAG = 18;
constexpr int TRA = 68;
constexpr int WMIN = 4, NLV = 6, LBASE = 3, LSH = 4, T8 = 110, T48 = 850, TAU = 1480;
constexpr int due(int n) { return n < 48 ? T8 + (T48 - T8) * (n - 8) / 40 : T48 + TAU * (n - 48) / 100; }
constexpr int TQ = 380, TH = 1010, DF = 1965, TF = 13;
constexpr int issued(int c) {
  const int a = c + 1 + LAG, f = c < WIN ? RING + c + 1 : RING + WIN + (c + 1 - WIN) / 3, m = a > f ? a : f;
  return m < 128 ? m : 128;
}
constexpr int DM = 576, FF = 4608;
constexpr u32 WS_EP = 0, WS_QP = 4096, WS_HP = WS_QP + 256 * 128 * 8, WS_ST = WS_HP + 216 * 128 * 8,
              WS_OP = WS_ST + 64 * 64 * 8, WS_FP = WS_OP + 256 * 128 * 8, WS_H2 = WS_FP + 256 * 144 * 8, WS_END = WS_H2 + 256 * 48 * 8;
constexpr int L_WQ = 0, L_W1 = 2 * 9216, L_WF = L_W1 + 192 * 128, L_SP = L_WF + 272 * 128, L_Q = L_SP + 272 * 16, L_ACC = L_Q + 256,
              L_A2 = L_ACC + 2048, L_AO = L_A2 + 4 * 320, L_ST = L_AO + 4 * 256, L_FW = L_ST + 64, L_CNT = L_FW + 16, L_G = L_CNT + 8, L_EP = L_G + 8, L_TB = L_EP + 8, L_OS = L_EP + 16, L_END = L_OS + 512;
constexpr u32 SPIN_CAP = 1u << 20;
typedef u32 u32x16 __attribute__((ext_vector_type(16)));
typedef u32 u32x8 __attribute__((ext_vector_type(8)));

__device__ __forceinline__ float bflo(u32 u) { return __uint_as_float(u << 16); }
__device__ __forceinline__ float bfhi(u32 u) { return __uint_as_float(u & 0xffff0000u); }
__device__ __forceinline__ u32 cvtpk(float a, float b) { bf16x2 c = {(__bf16)a, (__bf16)b}; return __builtin_bit_cast(u32, c); }
template <int C> __device__ __forceinline__ u32 dppu(u32 x) { return (u32)__builtin_amdgcn_update_dpp(0, (int)x, C, 0xf, 0xf, true); }
template <int C> __device__ __forceinline__ float dppf(float x) { return __uint_as_float(dppu<C>(__float_as_uint(x))); }
constexpr int X1 = 0xB1, X2 = 0x4E, X7 = 0x141, R8 = 0x128, R4 = 0x124, R2 = 0x122, R1 = 0x121;
__device__ __forceinline__ float sw16s(float x) { auto s = __builtin_amdgcn_permlane16_swap(__float_as_uint(x), __float_as_uint(x), false, false); return __uint_as_float(s[0]) + __uint_as_float(s[1]); }
__device__ __forceinline__ float sw32s(float x) { auto s = __builtin_amdgcn_permlane32_swap(__float_as_uint(x), __float_as_uint(x), false, false); return __uint_as_float(s[0]) + __uint_as_float(s[1]); }
__device__ __forceinline__ float sw16m(float x) { auto s = __builtin_amdgcn_permlane16_swap(__float_as_uint(x), __float_as_uint(x), false, false); return fmaxf(__uint_as_float(s[0]), __uint_as_float(s[1])); }
__device__ __forceinline__ float sw32m(float x) { auto s = __builtin_amdgcn_permlane32_swap(__float_as_uint(x), __float_as_uint(x), false, false); return fmaxf(__uint_as_float(s[0]), __uint_as_float(s[1])); }
__device__ __forceinline__ float row16s(float x) { x += dppf<X1>(x); x += dppf<X2>(x); x += dppf<X7>(x); x += dppf<R8>(x); return x; }
__device__ __forceinline__ float row16m(float x) { x = fmaxf(x, dppf<X1>(x)); x = fmaxf(x, dppf<X2>(x)); x = fmaxf(x, dppf<X7>(x)); x = fmaxf(x, dppf<R8>(x)); return x; }
__device__ __forceinline__ float rdl(float x, int l) { return __uint_as_float(__builtin_amdgcn_readlane(__float_as_uint(x), l)); }
__device__ __forceinline__ float dot2(u32 a, u32 b, float c) { return __builtin_amdgcn_fdot2_f32_bf16(__builtin_bit_cast(bf16x2, a), __builtin_bit_cast(bf16x2, b), c, false); }
__device__ __forceinline__ f32x4 mfma(u32x4 a, u32x4 b, f32x4 c) { return __builtin_amdgcn_mfma_f32_16x16x32_bf16(__builtin_bit_cast(bf16x8, a), __builtin_bit_cast(bf16x8, b), c, 0, 0, 0); }
__device__ __forceinline__ void wg_barrier() { asm volatile("s_waitcnt lgkmcnt(0)\n\ts_barrier" ::: "memory"); }
__device__ __forceinline__ void vm_drain() { asm volatile("s_waitcnt vmcnt(0)" ::: "memory"); }

template <int V> struct IC { static constexpr int v = V; };
template <int A, int B, class F> __device__ __forceinline__ void sfor(F&& f) { if constexpr (A < B) { f(IC<A>{}); sfor<A + 1, B>(f); } }
template <class T> __device__ __forceinline__ void tie(T& r) { asm volatile("" : "+v"(r)); }
template <int O> __device__ __forceinline__ u32x4 ld16(u64 base, u32 voff) { u32x4 r; asm volatile("global_load_dwordx4 %0, %1, %2 offset:%3" : "=v"(r) : "v"(voff), "s"(base), "i"(O) : "memory"); return r; }
template <int O> __device__ __forceinline__ u32 ld4(u64 base, u32 voff) { u32 r; asm volatile("global_load_dword %0, %1, %2 offset:%3" : "=v"(r) : "v"(voff), "s"(base), "i"(O) : "memory"); return r; }
__device__ __forceinline__ u32x2 ldep(u64 base) { u32x2 r; asm volatile("global_load_dwordx2 %0, %1, %2 sc1" : "=v"(r) : "v"(0), "s"(base) : "memory"); return r; }
__device__ __forceinline__ u32x4 srd(u64 base, u32 bytes) { return u32x4{(u32)base, (u32)(base >> 32) & 0xffffu, bytes, 0x00020000u}; }
__device__ __forceinline__ void dma16(u32x4 d, u32 voff, u32 soff, u32 lds) {
  asm volatile("s_nop 0\n\tbuffer_load_dwordx4 %0, %1, %2 offen nt lds" :: "v"(voff), "s"(d), "s"(soff), "{m0}"(lds) : "memory");
}
__device__ __forceinline__ void dma16k(u32x4 d, u32 voff, u32 soff, u32 lds) {
  asm volatile("s_nop 0\n\tbuffer_load_dwordx4 %0, %1, %2 offen lds" :: "v"(voff), "s"(d), "s"(soff), "{m0}"(lds) : "memory");
}
template <int T, int S = 0> __device__ __forceinline__ void not_before(u32& now, u32 tb) {
  asm volatile("s_cmp_ge_u32 %0, %c2\n\ts_cbranch_scc1 2f\n1:\n\ts_sleep %c3\n\ts_memrealtime vcc\n\ts_waitcnt lgkmcnt(0)\n\ts_sub_u32 %0, vcc_lo, %1\n\ts_cmp_lt_u32 %0, %c2\n\ts_cbranch_scc1 1b\n2:"
               : "+s"(now) : "s"(tb), "i"(T), "i"(S) : "scc", "vcc", "memory");
}
__device__ __forceinline__ u32x4 trb(LAS char* p, int rowbytes) {
  s16x4 a = __builtin_amdgcn_ds_read_tr16_b64_v4i16((LAS s16x4*)p);
  s16x4 b = __builtin_amdgcn_ds_read_tr16_b64_v4i16((LAS s16x4*)(p + 4 * rowbytes));
  u32x2 x = __builtin_bit_cast(u32x2, a), y = __builtin_bit_cast(u32x2, b);
  return u32x4{x[0], x[1], y[0], y[1]};
}
struct Tag { u32 lo, hi; };
__device__ __forceinline__ Tag mktag(u64 e) { return Tag{(u32)__builtin_amdgcn_readfirstlane((u32)e << 24), (u32)__builtin_amdgcn_readfirstlane((u32)(e >> 8))}; }
__device__ __forceinline__ void put_raw(GAS u64* p, u32 bits, Tag t) {
  __hip_atomic_store(p, ((u64)t.hi << 32) | (u64)((bits >> 8) | t.lo), __ATOMIC_RELAXED, __HIP_MEMORY_SCOPE_AGENT);
}
__device__ __forceinline__ void put(GAS u64* p, float v, Tag t) { put_raw(p, __float_as_uint(v) + 0x80u, t); }
__device__ __forceinline__ u64 peek(GAS u64* p) { return __hip_atomic_load(p, __ATOMIC_RELAXED, __HIP_MEMORY_SCOPE_AGENT); }
#define LAND "v240", "v241", "v242", "v243", "v244", "v245", "v246", "v247", "v248", "v249", "v250", "v251", "v252", "v253", "v254", "v255"
#define PL(a, b, o) "global_load_dwordx2 v[" #a ":" #b "], %[p], off offset:" o " sc1\n\t"
#define PL4(a, b, c, d, e, f, g, h) PL(a, b, "0") PL(c, d, "%c[s1]") PL(e, f, "%c[s2]") PL(g, h, "%c[s3]")
#define CK4(a, b, c, d, e, f, g, h) "v_bitop3_b32 %[r0], v" #b ", v" #d ", %[hi] bitop3:0x7e\n\tv_bitop3_b32 %[r1], v" #f ", v" #h ", %[hi] bitop3:0x7e\n\t" \
    "v_bitop3_b32 %[r2], v" #a ", v" #c ", %[lo] bitop3:0x7e\n\tv_bitop3_b32 %[r3], v" #e ", v" #g ", %[lo] bitop3:0x7e\n\t" \
    "v_or_b32 %[r2], %[r2], %[r3]\n\tv_lshrrev_b32 %[r2], 24, %[r2]\n\tv_or3_b32 %[r0], %[r0], %[r1], %[r2]\n\tv_cmp_ne_u32 vcc, 0, %[r0]\n\t" \
    "v_lshlrev_b32 %[r0], 8, v" #a "\n\tv_lshlrev_b32 %[r1], 8, v" #c "\n\tv_lshlrev_b32 %[r2], 8, v" #e "\n\tv_lshlrev_b32 %[r3], 8, v" #g "\n\t" \
    "s_and_b64 exec, exec, vcc\n\ts_cbranch_execz 2f\n\t"
#define SETA(M) M(240, 241, 242, 243, 244, 245, 246, 247)
#define SETB(M) M(248, 249, 250, 251, 252, 253, 254, 255)
#define PEND "s_sub_u32 %[n], %[n], 1\n\ts_cbranch_scc0 1b\n\tv_mov_b32 %[r0], -1\n2:\n\ts_mov_b64 exec, %[k]"
#define POUT [r0] "+v"(v[0]), [r1] "+v"(v[1]), [r2] "+v"(v[2]), [r3] "+v"(v[3]), [k] "=&s"(keep), [n] "=&s"(n)
#define PIN [p] "v"(p), [hi] "s"(t.hi), [lo] "s"(t.lo), [s1] "i"(S), [s2] "i"(2 * S), [s3] "i"(3 * S), [cap] "i"(SPIN_CAP)
template <int S> __device__ __forceinline__ void poll4_ahead(GAS u64* p) {
  asm volatile(SETA(PL4) :: [p] "v"(p), [s1] "i"(S), [s2] "i"(2 * S), [s3] "i"(3 * S) : "memory", LAND);
}
template <int S, int GAP> __device__ __forceinline__ void poll4x2(GAS u64* p, Tag t, u64 mask, float (&v)[4]) {
  u64 keep; u32 n;
  asm volatile("s_mov_b64 %[k], exec\n\ts_and_b64 exec, exec, %[m]\n\t" SETA(CK4) SETA(PL4) "s_mov_b32 %[n], %c[cap]\n\ts_sleep %c[gap]\n"
               "1:\n\t" SETB(PL4) "s_waitcnt vmcnt(4)\n\t" SETA(CK4) SETA(PL4) "s_waitcnt vmcnt(4)\n\t" SETB(CK4) PEND
               : POUT : PIN, [m] "s"(mask), [gap] "i"(GAP) : "vcc", "scc", "memory", LAND);
}
template <int IDLE> __device__ __forceinline__ float poll1(GAS u64* p, Tag t) {
  u64 keep; u32 n; float r;
  asm volatile("s_mov_b64 %[k], exec\n\ts_mov_b32 %[n], %c[cap]\n1:\n\t" PL(240, 241, "0") "s_waitcnt vmcnt(0)\n\t"
               "v_xor_b32 %[r0], %[lo], v240\n\tv_lshrrev_b32 %[r0], 24, %[r0]\n\tv_bitop3_b32 %[r0], %[r0], v241, %[hi] bitop3:0xf6\n\tv_cmp_ne_u32 vcc, 0, %[r0]\n\t"
               "v_lshlrev_b32 %[r0], 8, v240\n\ts_and_b64 exec, exec, vcc\n\ts_cbranch_execz 2f\n\ts_sleep %c[idle]\n\t" PEND
               : [r0] "=&v"(r), [k] "=&s"(keep), [n] "=&s"(n) : [p] "v"(p), [hi] "s"(t.hi), [lo] "s"(t.lo), [cap] "i"(SPIN_CAP), [idle] "i"(IDLE) : "vcc", "scc", "memory", LAND);
  return r;
}
__device__ __forceinline__ f32x4 sel4(const f32x4* c, int g) {
  f32x4 r;
#pragma unroll
  for (int e = 0; e < 4; ++e) { float a = (g & 1) ? c[1][e] : c[0][e], b = (g & 1) ? c[3][e] : c[2][e]; asm("" : "+v"(a), "+v"(b)); r[e] = (g & 2) ? b : a; }
  return r;
}
template <int N, int SLEEP> __device__ __forceinline__ void sweep(GAS u64* p, int stride, Tag t, float (&v)[N]) {
#pragma unroll 1
  for (u32 spin = 0;; ++spin) {
    u32 bh = 0, bl = 0;
#pragma unroll
    for (int k = 0; k < N; ++k) {
      const u64 x = peek(p + k * stride);
      bh |= (u32)(x >> 32) ^ t.hi; bl |= (u32)x ^ t.lo;
      v[k] = __uint_as_float((u32)x << 8);
    }
    v[0] = spin >= SPIN_CAP ? __uint_as_float(0x7fc00000u) : v[0];
    if (__builtin_amdgcn_ballot_w64((bh | (bl >> 24)) != 0) == 0 || spin >= SPIN_CAP) break;
    __builtin_amdgcn_s_sleep(SLEEP);
  }
}
__device__ __forceinline__ float rowsum4(const float (&v)[4]) {
  auto s0 = __builtin_amdgcn_permlane16_swap(__float_as_uint(v[0]), __float_as_uint(v[1]), false, false);
  auto s1 = __builtin_amdgcn_permlane16_swap(__float_as_uint(v[2]), __float_as_uint(v[3]), false, false);
  const float a = __uint_as_float(s0[0]) + __uint_as_float(s0[1]), c = __uint_as_float(s1[0]) + __uint_as_float(s1[1]);
  auto s2 = __builtin_amdgcn_permlane32_swap(__float_as_uint(a), __float_as_uint(c), false, false);
  return __uint_as_float(s2[0]) + __uint_as_float(s2[1]);
}

__global__ void __launch_bounds__(512, 2)
fused_kernel(const void* a_kc, const void* a_vc, const void* a_x, const void* a_wq, void* a_ws, const void* a_gw, const void* a_w1,
             const void* a_b1, const void* a_b2, const void* a_w2, const void* a_wo, void* a_out) {
  __shared__ __attribute__((aligned(16))) char lds_raw[L_END];
  LAS char* lds = (LAS char*)lds_raw;
  const u32 lds0 = (u32)(size_t)lds;
  u32x16 ka; u32x8 kb;
  u64 t0;
  asm volatile(".Lk_begin:\n\ts_load_dword s92, %0, 0x0\n\ts_load_dword s93, %0, 0x40" :: "s"(__builtin_amdgcn_kernarg_segment_ptr()) : "s92", "s93");
  __builtin_amdgcn_sched_barrier(0);
  const int tid = threadIdx.x, lane = tid & 63, wave = __builtin_amdgcn_readfirstlane(tid >> 6);
  const int bid = blockIdx.x, idx = bid >> 3, h = (bid & 7) * 4 + (idx >> 3), j = idx & 7, b = j >> 2, s = j & 3;
  const int g4 = lane >> 4, c16 = lane & 15;
  const int trl = (8 * g4 + (c16 >> 2)), trc = (lane & 3) * 8;
  const int swz = ((c16 >> 3) + (g4 & 1) * 2) * 2, t128 = trl * 128 + trc;
#define ARGS_IN() asm volatile("s_load_dwordx16 %0, %2, 0x0\n\ts_load_dwordx8 %1, %2, 0x40\n\ts_waitcnt lgkmcnt(0)" : "=&s"(ka), "=&s"(kb) : "s"(__builtin_amdgcn_kernarg_segment_ptr()))
#define ARG(v, i) (((u64)v[2 * (i) + 1] << 32) | v[2 * (i)])
#define kc ARG(ka, 0)
#define vc ARG(ka, 1)
#define xin ARG(ka, 2)
#define wq ARG(ka, 3)
#define ws ARG(ka, 4)
#define gw ARG(ka, 5)
#define w1 ARG(ka, 6)
#define b1 ARG(ka, 7)
#define b2 ARG(kb, 0)
#define w2 ARG(kb, 1)
#define wo ARG(kb, 2)
#define out ARG(kb, 3)
#define wsg ((GAS char*)ws)
  const u32 slab = ((b * 32 + h) * 4 + s) * 262144;

  if (__builtin_expect(wave >= 4, 1)) {
    const int sw = wave - 4;
    ARGS_IN();
    asm volatile("s_memrealtime %0" : "=s"(t0));
    const __amdgpu_buffer_rsrc_t rk = __builtin_amdgcn_make_buffer_rsrc((void*)(kc + slab), 0, 262144, 0x00020000);
    const __amdgpu_buffer_rsrc_t rv = __builtin_amdgcn_make_buffer_rsrc((void*)(vc + slab), 0, 262144, 0x00020000);
    const int vo = sw * 1024 + lane * 16;
    int so = idx * 4096;
    u32x4 ring[RING];
    u64 tab = 0, tgt = 0;
    u32 tb = 0;
#define RETUNE(n) do { u32 lo_, hi_; \
      asm volatile("s_memrealtime vcc\n\ts_waitcnt lgkmcnt(0)\n\ts_sub_u32 vcc_lo, vcc_lo, %2\n\ts_addk_i32 vcc_lo, %c5\n\ts_ashr_i32 vcc_lo, vcc_lo, %c6\n\t" \
                   "s_max_i32 vcc_lo, vcc_lo, 0\n\ts_min_i32 vcc_lo, vcc_lo, %c7\n\ts_lshl3_add_u32 %0, vcc_lo, %3\n\ts_addc_u32 %1, %4, 0" : "=&s"(lo_), "=&s"(hi_) \
                   : "s"(tb), "s"((u32)tab), "s"((u32)(tab >> 32)), "i"((LBASE << LSH) - due(n)), "i"(LSH), "i"(NLV - 1) : "scc", "vcc"); \
      tgt = ((u64)hi_ << 32) | lo_; } while (0)
#define ISSUE(n) do { if ((n) >= WIN) asm volatile("s_swappc_b64 vcc, %0" :: "s"(tgt) : "memory", "vcc"); \
      ring[(n) % RING] = __builtin_amdgcn_raw_buffer_load_b128((n) < 64 ? rk : rv, vo, so, 2); \
      so = (so + 4096) & 0x3ffff; asm volatile("" : "+s"(so)); if ((n) >= WIN && (n) % 4 == 0) RETUNE(n); __builtin_amdgcn_sched_barrier(0); } while (0)
    sfor<0, WIN>([&](auto N) __attribute__((always_inline)) { ISSUE(N.v); });
    asm volatile("s_waitcnt lgkmcnt(0)" : "+s"(t0));
    tb = (u32)t0;
    *(LAS u32*)(lds + L_TB) = tb;
    {
      asm volatile("s_getpc_b64 %0\n\ts_branch 1f\n\t.set wv, %c1\n\t.rept %c2\n\ts_waitcnt vmcnt(wv)\n\ts_setpc_b64 vcc\n\t.set wv, wv+1\n\t.endr\n1:" : "=s"(tab) : "i"(WMIN - 1), "i"(NLV));
      tab += 4; tgt = tab + (WIN - WMIN) * 8;
    }
    sfor<WIN, RING>([&](auto N) __attribute__((always_inline)) { ISSUE(N.v); });
    wg_barrier();
    const u32x4 q = *(LAS u32x4*)(lds + L_Q + c16 * 16);
    Tag tag = mktag(*(LAS u64*)(lds + L_EP));
    float sc[4], t8[8];
    sfor<0, 64>([&](auto N) __attribute__((always_inline)) {
      constexpr int n = N.v;
      const u32x4 k = ring[n % RING];
      float d = dot2(k[0], q[0], 0.f); d = dot2(k[1], q[1], d); d = dot2(k[2], q[2], d); d = dot2(k[3], q[3], d);
      if (issued(n) > issued(n - 1)) ISSUE(issued(n - 1));
      constexpr int i = n & 15;
      d += dppf<R8>(d);
      t8[i & 7] = (i < 8 || (c16 & 8)) ? d : t8[i & 7];
      if (i == 15) {
#pragma unroll
        for (int u = 0; u < 4; ++u) { const float lo = t8[u] + dppf<X7>(t8[u]), hi = t8[u + 4] + dppf<X7>(t8[u + 4]); t8[u] = (c16 & 4) ? hi : lo; }
#pragma unroll
        for (int u = 0; u < 2; ++u) { const float lo = t8[u] + dppf<X2>(t8[u]), hi = t8[u + 2] + dppf<X2>(t8[u + 2]); t8[u] = (c16 & 2) ? hi : lo; }
        { const float lo = t8[0] + dppf<X1>(t8[0]), hi = t8[1] + dppf<X1>(t8[1]); sc[n >> 4] = ((c16 & 1) ? hi : lo) * (0.08838834764831845f * 1.4426950408889634f); }
      }
      __builtin_amdgcn_sched_barrier(0);
    });
    float m = fmaxf(fmaxf(sc[0], sc[1]), fmaxf(sc[2], sc[3]));
    m = sw32m(sw16m(row16m(m)));
    u32 pk[4]; float l = 0.f;
#pragma unroll
    for (int u = 0; u < 4; ++u) { const float p = __builtin_amdgcn_exp2f(sc[u] - m); pk[u] = cvtpk(p, p); l += bflo(pk[u]); }
    l = sw32s(sw16s(row16s(l)));
    if (lane == 0) {
      const u32 mb = __float_as_uint(m), lb = __float_as_uint(l) + 0x80u;
      *(LAS u32x4*)(lds + L_ST + sw * 16) = u32x4{mb, (mb & 0xffu) << 8, lb, lb};
    }
    u32 mk[4];
#pragma unroll
    for (int u = 0; u < 4; ++u) mk[u] = (c16 >> 1) == u ? ((c16 & 1) ? 0xffff0000u : 0x0000ffffu) : 0u;
    f32x4 acc = {0.f, 0.f, 0.f, 0.f};
    float fw = 0.f;
    GAS u64* op = (GAS u64*)(wsg + WS_OP) + (h * 8 + j) * 128 + lane;
    GAS u64* fp = (GAS u64*)(wsg + WS_FP) + (j * 32 + h) * 144 + (lane < 32 ? 16 * sw + c16 : 64 + c16) * 2 + (g4 & 1);
    u32 lacc = lds0 + L_ACC + sw * 512 + (g4 * 8 + c16) * 16, lsum = lds0 + L_ACC + lane * 4, lao = lds0 + L_AO + (lane & 3) * 256 + g4 * 16;
    tie(op); tie(fp); tie(lacc); tie(lsum); tie(lao);
    asm volatile("" : "+s"(tag.lo), "+s"(tag.hi));
#define PV(n) do { const u32 pb_ = pk[((n) - 64) >> 4]; constexpr int C_ = 0x150 + (((n) - 64) & 15); \
      const u32x4 B_ = {dppu<C_>(pb_) & mk[0], dppu<C_>(pb_) & mk[1], dppu<C_>(pb_) & mk[2], dppu<C_>(pb_) & mk[3]}; \
      acc = mfma(ring[(n) % RING], B_, acc); __builtin_amdgcn_sched_barrier(0); \
      if (issued(n) > issued((n) - 1)) { ISSUE(issued((n) - 1)); if (issued((n) - 1) == 127) { __builtin_amdgcn_s_setprio(3); wg_barrier(); fw = *(LAS float*)(lds + L_FW + sw * 4); __builtin_amdgcn_sched_barrier(0); } } if ((n) == TRA) wg_barrier(); } while (0)
#define PV4(n) PV(n); PV((n) + 1); PV((n) + 2); PV((n) + 3)
#define PV16(n) PV4(n); PV4((n) + 4); PV4((n) + 8); PV4((n) + 12)
    PV16(64); PV16(80); PV16(96); PV16(112);
    acc *= fw;
    if (c16 < 8) *(LAS f32x4*)(size_t)lacc = acc;
    u32 arrived;
    asm volatile("s_mov_b64 exec, 1\n\tds_add_rtn_u32 %0, %1, %2\n\ts_mov_b64 exec, -1\n\ts_waitcnt lgkmcnt(0)" : "=v"(arrived) : "v"(lds0 + L_CNT), "v"(1u) : "memory");
    if (__builtin_amdgcn_readfirstlane(arrived) == 3) {
      LAS float* a = (LAS float*)(size_t)lsum;
      const float o0 = (a[0] + a[128]) + (a[256] + a[384]), o1 = (a[64] + a[192]) + (a[320] + a[448]);
      put(op, o0, tag); put(op + 64, o1, tag);
      a[(L_OS - L_ACC) / 4] = o0; a[(L_OS - L_ACC) / 4 + 64] = o1;
    }
    wg_barrier();
    f32x4 c0 = {0.f, 0.f, 0.f, 0.f}, c1 = c0, c2 = c0, c3 = c0;
    LAS char* wb = lds + L_WF + t128 + (((2 * sw) ^ swz) << 4);
    LAS char* sb = lds + L_SP + trl * 16 + (trc & 8);
#pragma unroll
    for (int ks = 0; ks < 5; ++ks) {
      const u32x4 a = *(LAS u32x4*)(lds + L_A2 + (lane & 3) * 320 + ks * 64 + g4 * 16);
      c0 = mfma(a, trb(wb + ks * 4096, 128), c0); c1 = mfma(a, trb(sb + ks * 512, 16), c1);
    }
    u32x4 bo[4], bs[4];
#pragma unroll
    for (int ks = 0; ks < 4; ++ks) { bo[ks] = trb(wb + 144 * 128 + ks * 4096, 128); bs[ks] = trb(sb + 144 * 16 + ks * 512, 16); }
    wg_barrier();
    u32x4 ao[4];
#pragma unroll
    for (int ks = 0; ks < 4; ++ks) ao[ks] = *(LAS u32x4*)(size_t)(lao + ks * 64);
    __builtin_amdgcn_sched_barrier(0);
    c0 = mfma(ao[0], bo[0], c0); c1 = mfma(ao[0], bs[0], c1); c2 = mfma(ao[1], bo[1], c2); c3 = mfma(ao[1], bs[1], c3);
    c0 = mfma(ao[2], bo[2], c0); c1 = mfma(ao[2], bs[2], c1); c2 = mfma(ao[3], bo[3], c2); c3 = mfma(ao[3], bs[3], c3);
    const f32x4 c = lane < 32 ? c0 + c2 : c1 + c3;
    if (lane < 32 || (sw == 0 && c16 < 8)) put(fp, (g4 & 1) ? c[1] + c[3] : c[0] + c[2], tag);
    return;
  }

  const int hw = wave, t = tid;
  u32 tb = 0;

  const int jq = bid & 7;
  const u32 vsw = ((lane & 7) ^ ((g4 & 1) * 2)) * 16;
  const int fu = bid < 144 ? bid : 0, fj = fu / 18, fc = 4 * (fu % 18) + ((lane & 7) >> 1), ncol = fc < 64 ? 64 * fj + fc : 448 + 8 * fj + fc;
  if (__builtin_expect(hw < 2, 1)) {
    const int kq = jq >> 1, cq = (jq & 1) * 32 + idx;
    u32x4 xa[5];
    ARGS_IN();
    asm volatile("s_memrealtime %0" : "=s"(t0));
    if (hw == 0) {
      const u32x4 d = srd(wq, DM * 4096 * 2);
      const u32 v = (lane >> 3) * 8192 + vsw, so = 144 * kq * 8192 + 128 * cq, l = lds0 + L_WQ;
#pragma unroll 1
      for (int u = 0; u < 9; ++u) { dma16(d, v, so + u * 131072, l + u * 2048); dma16(d, v ^ 64, so + u * 131072 + 65536, l + u * 2048 + 1024); }
    }
    const u32 vq = (lane & 1) * (DM * 2) + 288 * kq;
    xa[0] = ld16<0>(xin, vq + 16 * g4); xa[1] = ld16<64>(xin, vq + 16 * g4); xa[2] = ld16<128>(xin, vq + 16 * g4); xa[3] = ld16<192>(xin, vq + 16 * g4);
    xa[4] = ld16<256>(xin, vq + 16 * (g4 & 1));
    u32x2 ep = ldep(ws);
    asm volatile("s_waitcnt lgkmcnt(0)" : "+s"(t0));
    tb = (u32)t0;
    vm_drain();
    tie(xa[0]); tie(xa[1]); tie(xa[2]); tie(xa[3]); tie(xa[4]); tie(ep);
    const u64 epn = (((u64)ep[1] << 32) | ep[0]) + 1;
    const Tag tag = mktag(epn);
    if (hw == 0) {
#pragma unroll
      for (int e = 0; e < 4; ++e) xa[4][e] = g4 < 2 ? xa[4][e] : 0u;
      f32x4 c[4];
#pragma unroll
      for (int i = 0; i < 4; ++i) {
        LAS char* p = lds + L_WQ + (((2 * i) ^ swz) << 4);
        c[i] = f32x4{0.f, 0.f, 0.f, 0.f};
#pragma unroll
        for (int ks = 0; ks < 4; ++ks) c[i] = mfma(xa[ks], trb(p + t128 + ks * 4096, 128), c[i]);
        c[i] = mfma(xa[4], trb(p + (t128 & 2047) + 16384, 128), c[i]);
      }
      const f32x4 cc = sel4(c, g4);
      GAS u64* qp = (GAS u64*)(wsg + WS_QP) + (cq * 4 + kq) * 128 + lane;
      put(qp, cc[0], tag); put(qp + 64, cc[1], tag);
      *(LAS u64*)(lds + L_EP) = epn;
    }
    { u32 now = 0; not_before<TQ, 2>(now, tb); }
    float pv[4];
    sweep<4, 12>((GAS u64*)(wsg + WS_QP) + (2 * h + hw) * 512 + b * 64 + lane, 128, tag, pv);
    const float qv = (pv[0] + pv[1]) + (pv[2] + pv[3]);
    *(LAS u16*)(lds + L_Q + t * 2) = (u16)cvtpk(qv, qv);
    wg_barrier();
  } else if (hw == 2) {
    ARGS_IN();
    {
      u64 pc; u32 back, len;
      asm volatile("s_getpc_b64 %0\n.Lk_here:\n\ts_mov_b32 %1, .Lk_here-.Lk_begin\n\ts_mov_b32 %2, .Lk_end-.Lk_begin-16" : "=s"(pc), "=s"(back), "=s"(len));
      u32x4 p0 = ld16<0>(pc - back, min((u32)(idx * 1024 + lane * 16), len));
      u32 p1 = ld4<0>(vc, slab + idx * 4096), p2 = ld4<0>(w2, 144 * h * (DM * 2)), p3 = ld4<0>(wo, 128 * h * (DM * 2)), p4 = ld4<0>(out, 0);
      { u64 z = peek((GAS u64*)(wsg + (lane < 9 ? WS_FP + (j * 32 + h) * 1152 + lane * 128 : WS_OP + (h * 8 + j) * 1024 + (lane & 7) * 128))); tie(z); }
      vm_drain();
      tie(p0); tie(p1); tie(p2); tie(p3); tie(p4);
    }
    if (lane < 33) *(LAS u32*)(lds + (lane < 32 ? L_A2 + (lane >> 3) * 320 + 288 + (lane & 7) * 4 : L_CNT)) = 0u;
    wg_barrier();
    if (lane < 8) {
      const u32x4 d2 = srd(w2 + 144 * h * (DM * 2), 144 * (DM * 2)), d3 = srd(wo + 128 * h * (DM * 2), 128 * (DM * 2));
      const u32 v = lane * (DM * 2) + 1024 + 16 * j, l = lds0 + L_SP;
#pragma unroll 1
      for (int u = 0; u < 144 / 8; ++u) { dma16k(d2, v, u * 8 * (DM * 2), l + u * 8 * 16); __builtin_amdgcn_s_sleep(4); }
#pragma unroll 1
      for (int u = 0; u < 128 / 8; ++u) { dma16k(d3, v, u * 8 * (DM * 2), l + 144 * 16 + u * 8 * 16); __builtin_amdgcn_s_sleep(4); }
    }
  } else {
    const int unit = jq * 27 + min(idx, 26), k3 = unit / 72, cb = unit % 72;
    const u32 vh = (lane & 1) * (DM * 2) + 384 * k3 + 16 * g4;
    u32x4 xa[6], xs[2], gs[2];
    ARGS_IN();
    const u32 vs = (lane >> 5) * (DM * 2), l31 = lane & 31;
    xs[0] = ld16<0>(xin, vs + l31 * 16); xs[1] = ld16<512>(xin, vs + l31 * 16); u32 xs2 = ld4<1024>(xin, vs + l31 * 4);
    gs[0] = ld16<0>(gw, l31 * 16); gs[1] = ld16<512>(gw, l31 * 16); u32 gs2 = ld4<1024>(gw, l31 * 4);
    sfor<0, 6>([&](auto K) __attribute__((always_inline)) { xa[K.v] = ld16<64 * K.v>(xin, vh); });
    vm_drain();
    tie(xs[0]); tie(xs[1]); tie(xs2); tie(gs[0]); tie(gs[1]); tie(gs2); tie(xa[0]); tie(xa[1]); tie(xa[2]); tie(xa[3]); tie(xa[4]); tie(xa[5]);
    __builtin_amdgcn_s_sleep(30);
    if (idx < 27) {
      const u32x4 d1 = srd(w1, DM * FF * 2);
      const u32 v = (lane >> 3) * (FF * 2) + vsw, so = 192 * k3 * (FF * 2) + 128 * cb, l = lds0 + L_W1;
#pragma unroll 1
      for (int u = 0; u < 12; ++u) { dma16(d1, v, so + u * 16 * (FF * 2), l + u * 2048); dma16(d1, v ^ 64, so + (u * 16 + 8) * (FF * 2), l + u * 2048 + 1024); }
    }
    float sum = bflo(xs2) + bfhi(xs2), gd = dot2(xs2, gs2, 0.f);
#pragma unroll
    for (int i = 0; i < 8; ++i) { const u32 u = xs[i >> 2][i & 3]; sum += bflo(u) + bfhi(u); gd = dot2(u, gs[i >> 2][i & 3], gd); }
    sum = sw16s(row16s(sum)); gd = sw16s(row16s(gd));
    const float mean = sum * (1.f / DM);
    float var = 0.f;
#pragma unroll
    for (int i = 0; i < 9; ++i) { const u32 u = i < 8 ? xs[(i >> 2) & 1][i & 3] : xs2; const float a = bflo(u) - mean, c = bfhi(u) - mean; var += a * a + c * c; }
    var = sw16s(row16s(var));
    const float rstd = __builtin_amdgcn_rsqf(var * (1.f / DM) + 1e-5f);
    *(LAS float*)(lds + L_G + (lane >> 5) * 4) = __builtin_amdgcn_rcpf(1.f + __expf(-gd));
    const float m0 = rdl(mean, 0), m1 = rdl(mean, 32), r0 = rdl(rstd, 0), r1 = rdl(rstd, 32);
    const float mu = (lane & 1) ? m1 : m0, rs = (lane & 1) ? r1 : r0;
#pragma unroll
    for (int ks = 0; ks < 6; ++ks)
#pragma unroll
      for (int e = 0; e < 4; ++e) {
        const float y0 = (bflo(xa[ks][e]) - mu) * rs, y1 = (bfhi(xa[ks][e]) - mu) * rs;
        const u32 hi = cvtpk(y0, y1), lo = cvtpk(y0 - bflo(hi), y1 - bfhi(hi));
        xa[ks][e] = (lane & 2) ? lo : hi;
      }
    vm_drain();
    f32x4 c[4];
#pragma unroll
    for (int i = 0; i < 4; ++i) {
      LAS char* p = lds + L_W1 + t128 + (((2 * i) ^ swz) << 4);
      c[i] = f32x4{0.f, 0.f, 0.f, 0.f};
#pragma unroll
      for (int ks = 0; ks < 6; ++ks) c[i] = mfma(xa[ks], trb(p + ks * 4096, 128), c[i]);
    }
    const f32x4 cc = sel4(c, g4);
    float z0 = cc[0] + cc[2], z1 = cc[1] + cc[3];
    tie(z0); tie(z1);
    wg_barrier();
    const Tag tag = mktag(*(LAS u64*)(lds + L_EP));
    if (idx < 27) { GAS u64* hp = (GAS u64*)(wsg + WS_HP) + (cb * 3 + k3) * 128 + lane; put(hp, z0, tag); put(hp + 64, z1, tag); }
    {
      const int f = 144 * h + 36 * s + min(lane, 35);
      const u32 bias = ((const GAS u16*)b1)[f];
      const float gate = *(LAS float*)(lds + L_G + b * 4);
      u32 now = 0;
      not_before<TH, 2>(now, __builtin_amdgcn_readfirstlane(*(LAS u32*)(lds + L_TB)));
      float v[3];
      sweep<3, 32>((GAS u64*)(wsg + WS_HP) + (f >> 6) * 384 + b * 64 + (f & 63), 128, tag, v);
      const float zz = (v[0] + v[1]) + (v[2] + bflo(bias));
      const float u2 = 1.5957691216057308f * (zz + 0.044715f * zz * zz * zz);
      if (lane < 48) put((GAS u64*)(wsg + WS_H2) + (h * 8 + j) * 48 + lane, (1.f - gate) * zz * __builtin_amdgcn_rcpf(1.f + __expf(-u2)), tag);
    }
  }
  const u64 epn = *(LAS u64*)(lds + L_EP);
  tb = __builtin_amdgcn_readfirstlane(*(LAS u32*)(lds + L_TB));
  const Tag tag = mktag(epn);
  const u32 bias2 = ((const GAS u16*)b2)[ncol];
  wg_barrier();
  const float gate0 = *(LAS float*)(lds + L_G), gate1 = *(LAS float*)(lds + L_G + 4);
  GAS u64* st = (GAS u64*)(wsg + WS_ST) + (b * 32 + h) * 64;
  if (t < 16) put_raw(st + s * 16 + lane, *(LAS u32*)(lds + L_ST + lane * 4), tag);
  const u32x4 d2 = srd(w2 + 144 * h * (DM * 2), 144 * (DM * 2)), d3 = srd(wo + 128 * h * (DM * 2), 128 * (DM * 2));
  {
    float v[2];
    const int i = min(lane, 35);
    sweep<2, 32>((GAS u64*)(wsg + WS_H2) + (h * 8 + 2 * hw) * 48 + i, 48, tag, v);
#pragma unroll
    for (int k = 0; k < 2; ++k) {
      const u32 hi = cvtpk(v[k], v[k]), lo = cvtpk(v[k] - bflo(hi), 0.f);
      LAS char* p = lds + L_A2 + (hw >> 1) * 32 + (72 * hw + 36 * k + i) * 2;
      *(LAS u16*)p = (u16)hi; *(LAS u16*)(p + 640) = (u16)lo;
    }
  }
  u32 vf = (lane >> 3) * (DM * 2) + (vsw ^ ((hw & 1) * 64));
  const int e = t & 127, bb = t >> 7, ob = lane & 1;
  GAS u64* op = (GAS u64*)(wsg + WS_OP) + (h * 8 + bb * 4 + g4) * 128 + (e & 64) + c16;
  GAS u64* fp = (GAS u64*)(wsg + WS_FP) + (fj * 32 + (lane >> 3)) * 144 + fc * 2 + ob;
  u32 lao = L_AO + bb * 256 + ((e & 96) + (e & 3) * 8 + ((e >> 2) & 7)) * 2;
  float fin = (1.f - (ob ? gate1 : gate0)) * bflo(bias2);
  u32 vout = (ob * DM + ncol) * 2;
  u32 los = lds0 + L_OS + ((e & 64) + c16) * 4;
  tie(los); tie(vf); tie(op); tie(fp); tie(lao); tie(fin); tie(vout);
  if (hw == 0) {
    const u32 r = __float_as_uint(poll1<16>(st + lane, tag));
    const float mw = __uint_as_float(dppu<0x00>(r) | (dppu<0x55>(r) >> 8)), lw = __uint_as_float(dppu<0xAA>(r));
    float M = fmaxf(mw, dppf<R4>(mw)); M = fmaxf(M, dppf<R8>(M)); M = sw32m(sw16m(M));
    const float ex = __builtin_amdgcn_exp2f(mw - M);
    float L = lw * ex; L += dppf<X7>(L); L += dppf<R8>(L); L = sw32s(sw16s(L));
    if (g4 == s && !(lane & 3)) *(LAS float*)(lds + L_FW + c16) = ex * (b ? gate1 : gate0) * __builtin_amdgcn_rcpf(L);
  }
  const u64 omask = (hw >> 1) == b ? ~(0xffffull << (16 * s)) : ~0ull;
  wg_barrier();
  {
    constexpr u32 G = 8 * DM * 2;
    const u32 so = hw * G + 128 * j, l = lds0 + L_WF + hw * 1024;
    u32 now = 0;
    sfor<0, 4>([&](auto I) __attribute__((always_inline)) { not_before<DF + I.v * TF>(now, tb); if (I.v) __builtin_amdgcn_s_sleep(4); dma16(d2, vf, so + I.v * 4 * G, l + I.v * 4096); });
    if (hw < 2) dma16(d2, vf, so + 16 * G, l + 16384);
    sfor<0, 4>([&](auto I) __attribute__((always_inline)) { not_before<DF + (4 + I.v) * TF>(now, tb); __builtin_amdgcn_s_sleep(4); dma16(d3, vf, so + I.v * 4 * G, l + 144 * 128 + I.v * 4096); });
  }
  vm_drain();
  poll4_ahead<128>(op);
  wg_barrier();
  {
    LAS float* own = (LAS float*)(size_t)los;
    float v[4] = {own[0], own[16], own[32], own[48]};
    vm_drain();
    poll4x2<128, 16>(op, tag, omask, v);
    const float o = rowsum4(v);
    const u32 hi = cvtpk(o, o), lo = cvtpk(o - bflo(hi), 0.f);
    *(LAS u16*)(lds + lao) = (u16)hi; *(LAS u16*)(lds + lao + 512) = (u16)lo;
  }
  wg_barrier();
  if (hw == 0 && bid < 144) {
    __builtin_amdgcn_s_sleep(16);
    float v[4];
    sweep<4, 0>(fp, 8 * 144, tag, v);
    float r = (v[0] + v[1]) + (v[2] + v[3]);
    r += dppf<R8>(r); r = sw32s(sw16s(r)) + fin;
    if (lane < 8) __hip_atomic_store((GAS u16*)((GAS char*)out + vout), (u16)cvtpk(r, r), __ATOMIC_RELAXED, __HIP_MEMORY_SCOPE_SYSTEM);
    if (bid == 0 && lane == 0) __hip_atomic_store((GAS u64*)(wsg + WS_EP), epn, __ATOMIC_RELAXED, __HIP_MEMORY_SCOPE_AGENT);
  }
  asm volatile(".Lk_end:");
}

extern "C" void kernel_launch(void* const* d_in, const int* in_sizes, int n_in, void* d_out, int out_size, void* d_ws,
                              size_t ws_size, hipStream_t stream) {
  fused_kernel<<<256, 512, 0, stream>>>(d_in[1], d_in[2], d_in[0], d_in[3], d_ws, d_in[10], d_in[6], d_in[7], d_in[9], d_in[8], d_in[4], d_out);
}
```
